```python
import torch

def _softmax(x, axis=-1):
    return torch.softmax(x, dim=axis)
def _gelu(x):
    return torch.nn.functional.gelu(x, approximate="tanh")
def _silu(x):
    return torch.nn.functional.silu(x)
def _sigmoid(x):
    return torch.sigmoid(x)
def _layernorm(x, axis=-1, eps=1e-5):
    m = x.mean(dim=axis, keepdim=True)
    v = x.var(dim=axis, keepdim=True, unbiased=False)
    return (x - m) / torch.sqrt(v + eps)
def _scatter_add(tgt, idx, src, axis=0):
    return tgt.clone().index_add_(axis, idx, src)
def _segment_sum(src, seg, n):
    out = torch.zeros((n,) + src.shape[1:], dtype=src.dtype)
    return out.index_add_(0, seg, src)
def _pad(x, pw):
    flat = tuple(v for pair in reversed(pw) for v in pair)
    return torch.nn.functional.pad(x, flat)

kernel_name = 'recurrent_scan__sliding_block__geglu_ffn__prompt'

def setup_inputs():

    _w = (lambda a: (lambda t: t.to(torch.bfloat16) if t.is_floating_point() else t)(torch.as_tensor(a)))
    def _A_setup():
        B, S, D = 1, 2048, 448
        return {
            "x": _w((lambda *s: torch.randn(*s, dtype=torch.bfloat16) * 0.05)(B, S, D)),
            "wqkv": _w((lambda *s: torch.randn(*s, dtype=torch.bfloat16) * 0.05)(D, 3 * D)),
            "wo": _w((lambda *s: torch.randn(*s, dtype=torch.bfloat16) * 0.05)(D, D)),
        }
    _A_in = _A_setup()

    def _B_setup():
        B, S, D, F = 1, 2048, 448, 1792
        return {
            "x": _w((lambda *s: torch.randn(*s, dtype=torch.bfloat16) * 0.05)(B, S, D)),
            "w_gate": _w((lambda *s: torch.randn(*s, dtype=torch.bfloat16) * 0.05)(D, F)),
            "w_up": _w((lambda *s: torch.randn(*s, dtype=torch.bfloat16) * 0.05)(D, F)),
            "w_down": _w((lambda *s: torch.randn(*s, dtype=torch.bfloat16) * 0.05)(F, D)),
        }
    _B_in = _B_setup()

    return {
        "A_x": _A_in["x"],
        "A_wqkv": _A_in["wqkv"],
        "A_wo": _A_in["wo"],
        "B_x": _B_in["x"],
        "B_w_gate": _B_in["w_gate"],
        "B_w_up": _B_in["w_up"],
        "B_w_down": _B_in["w_down"],
    }

def reference(A_x, A_wqkv, A_wo, B_x, B_w_gate, B_w_up, B_w_down):
    def _A_ref(x, wqkv, wo):
        B, S, D = x.shape
        H = 16; Dh = D // H; W = 256
        qkv = (lambda x, *d: x.permute(*d))((x @ wqkv).reshape(B, S, 3, H, Dh), 2, 0, 3, 1, 4)
        q, k, v = qkv[0], qkv[1], qkv[2]
        scores = torch.einsum("bhqd,bhkd->bhqk", q, k) * (Dh ** -0.5)
        idx = torch.arange(S)
        mask = (idx[:, None] - idx[None, :] >= 0) & (idx[:, None] - idx[None, :] < W)
        scores = torch.where(mask[None, None], scores, -1e9)
        p = _softmax(scores, axis=-1)
        o = torch.einsum("bhqk,bhkd->bhqd", p, v)
        return (lambda x, *d: x.permute(*d))(o, 0, 2, 1, 3).reshape(B, S, D) @ wo

    def _B_ref(x, w_gate, w_up, w_down):
        return (_gelu(x @ w_gate) * (x @ w_up)) @ w_down

    x = A_x
    xn = _layernorm(x, axis=-1)
    s = _A_ref(xn, A_wqkv, A_wo)
    g = _silu(_B_ref(xn, B_w_gate, B_w_up, B_w_down))
    return x + s * g
```

```cpp
#include <hip/hip_runtime.h>
#include <hip/hip_bf16.h>

typedef __attribute__((ext_vector_type(8))) __bf16 bf16x8;
typedef __attribute__((ext_vector_type(2))) __bf16 bf16x2;
typedef __attribute__((ext_vector_type(16))) float f32x16;
typedef __attribute__((ext_vector_type(4))) float f32x4;
typedef __attribute__((ext_vector_type(2))) float f32x2;
typedef __attribute__((ext_vector_type(4))) unsigned u32x4;
typedef __attribute__((ext_vector_type(2))) unsigned u32x2;
typedef short v4i16_t __attribute__((ext_vector_type(4)));
typedef short v8i16_t __attribute__((ext_vector_type(8)));
typedef unsigned long long u64;
#define LAS __attribute__((address_space(3)))
typedef LAS char* lds_ptr;
typedef __attribute__((address_space(1))) u64 gu64;
#define DI __device__ __forceinline__
#define MFMA(a, b, c) __builtin_amdgcn_mfma_f32_32x32x16_bf16(a, b, c, 0, 0, 0)
#define RLX_AGENT __ATOMIC_RELAXED, __HIP_MEMORY_SCOPE_AGENT
#define WAITV(N) asm volatile("s_waitcnt vmcnt(" #N ")" ::: "memory")
#define BAR() asm volatile("s_waitcnt lgkmcnt(0)\n\ts_barrier" ::: "memory")
#define WAITV7(N) do { if (w == 7) WAITV(0); else WAITV(N); } while (0)

#define STAMPS 0

constexpr int AUX_SC1 = 16;
constexpr unsigned WS_EPOCH = 0;
constexpr unsigned WS_KVF = 4096;
constexpr unsigned WS_OF = WS_KVF + 65536;
constexpr unsigned WS_HF = WS_OF + 65536;
constexpr unsigned WS_STAMP = 512 * 1024;
constexpr unsigned WS_KV = 1u << 20,
                    WS_O = 7u << 20, WS_H = 9u << 20, WS_END = 16u << 20;
constexpr int L_R = 0, L_S0 = 48640, L_GS = 28800, L_UP = 14336 + 128;
constexpr int L_Q = 32768, L_KV = 6 * 16384, L_SCR = 9 * 16384;
constexpr int LDS_BYTES = 163840;
DI constexpr int gslot(int i) { return L_S0 + i * L_GS; }
DI constexpr int pslot(int s) { return (s % 10 < 2 ? s % 10 : s % 10 < 8 ? s % 10 + 1 : s % 10 == 8 ? 2 : 9) * 16384; }

struct Params {
    const char* x; const char* wqkv; const char* wo; const char* wg; const char* wu; const char* wd;
    char* out; char* ws;
};

DI void glds(const char* sbase, unsigned voff, unsigned lds) {
    unsigned keep;
    asm volatile("s_mov_b32 %0, m0\n\ts_mov_b32 m0, %3\n\ts_nop 0\n\tglobal_load_lds_dwordx4 %1, %2\n\ts_mov_b32 m0, %0"
                 : "=&s"(keep) : "v"(voff), "s"(sbase), "s"(lds) : "memory");
}
DI v4i16_t trr(lds_ptr p) { return __builtin_amdgcn_ds_read_tr16_b64_v4i16((LAS v4i16_t*)p); }
DI bf16x8 trfrag(lds_ptr p0, lds_ptr p1) {
    v4i16_t a = trr(p0), b = trr(p1);
    return __builtin_bit_cast(bf16x8, __builtin_shufflevector(a, b, 0, 1, 2, 3, 4, 5, 6, 7));
}
DI unsigned pk2(float a, float b) { f32x2 v = {a, b}; return __builtin_bit_cast(unsigned, __builtin_convertvector(v, bf16x2)); }
DI u32x4 pack8(const f32x16& c, int s) {
    u32x4 r = {pk2(c[8 * s], c[8 * s + 1]), pk2(c[8 * s + 2], c[8 * s + 3]), pk2(c[8 * s + 4], c[8 * s + 5]), pk2(c[8 * s + 6], c[8 * s + 7])};
    return r;
}
DI float bflo(unsigned u) { return __uint_as_float(u << 16); }
DI float bfhi(unsigned u) { return __uint_as_float(u & 0xffff0000u); }
DI float rbf(float a) { return bflo(pk2(a, a) & 0xffffu); }
DI float xhalf(float v) {
    auto r = __builtin_amdgcn_permlane32_swap(__float_as_uint(v), __float_as_uint(v), false, false);
    return __uint_as_float((threadIdx.x & 32) ? r[0] : r[1]);
}
DI u32x4 ldws(__amdgpu_buffer_rsrc_t rs, unsigned off) { return __builtin_amdgcn_raw_buffer_load_b128(rs, (int)off, 0, AUX_SC1); }
DI void stws(__amdgpu_buffer_rsrc_t rs, unsigned off, u32x4 v) { __builtin_amdgcn_raw_buffer_store_b128(v, rs, (int)off, 0, AUX_SC1); }
DI void set_flag(char* ws, unsigned off, u64 tag, int lane) {
    if (lane == 0) __hip_atomic_store((gu64*)(ws + off), tag, RLX_AGENT);
}
DI void wait_flags(char* ws, unsigned off, u64 tag) {
    for (int spin = 0; spin < (1 << 20); ++spin) {
        u64 v = __hip_atomic_load((gu64*)(ws + off), RLX_AGENT);
        if (__all(v == tag)) break;
        __builtin_amdgcn_s_sleep(2);
    }
    __builtin_amdgcn_fence(__ATOMIC_ACQUIRE, "wavefront");
}
DI u64 poll_issue(char* ws, unsigned off) {
    u64 v;
    asm volatile("global_load_dwordx2 %0, %1, %2 sc1" : "=v"(v) : "v"(off), "s"(ws) : "memory");
    return v;
}
#define POLL_CHECK(N, v, off) do { asm volatile("s_waitcnt vmcnt(" #N ")" : "+v"(v) :: "memory"); if (!__all(v == tag)) wait_flags(p.ws, off, tag); } while (0)
#define STAMP(i)
#define STAMP_DECL
#define STAMP_OUT

#define SBAR() __builtin_amdgcn_sched_barrier(0)
#define RAWBAR() asm volatile("s_barrier" ::: "memory")
template <int V> struct IC { static constexpr int v = V; constexpr operator int() const { return V; } };
template <int I, int N, class F>
DI void sfor(F&& f) { if constexpr (I < N) { f(IC<I>{}); sfor<I + 1, N>(f); } }
template <int N, int D, class LD, class MM>
DI void pipe(LD ld, MM mm) {
    bf16x8 buf[D];
    sfor<0, D - 1>([&](auto i) { buf[i.v] = ld(i); });
    SBAR();
    sfor<0, N>([&](auto i) {
        if constexpr (i.v + D - 1 < N) buf[(i.v + D - 1) % D] = ld(IC<i.v + D - 1>{});
        SBAR();
        mm(i, buf[i.v % D]);
        SBAR();
    });
}
DI float gelu_mul(float g, float u) {
    const float c1 = -2.0f * 0.7978845608028654f * 1.4426950408889634f, c2 = c1 * 0.044715f;
    float e = __builtin_amdgcn_exp2f(g * __builtin_fmaf(g * g, c2, c1));
    return g * u * __builtin_amdgcn_rcpf(1.0f + e);
}

__global__ __launch_bounds__(512, 2) void fused_kernel(Params p) {
    extern __shared__ __attribute__((aligned(16))) char smem_[];
    lds_ptr sm = (lds_ptr)smem_;
    const unsigned smb = (unsigned)(size_t)sm;
    const int tid = threadIdx.x, lane = tid & 63;
    const int w = __builtin_amdgcn_readfirstlane(tid >> 6);
    const int hi = lane >> 5;
    const int xcd = blockIdx.x & 7, li = blockIdx.x >> 3;
    const int rt = 4 * (xcd >> 1) + (li >> 3), g = 8 * (xcd & 1) + (li & 7);
    const bool p3 = li < 28;
    const int rb = 4 * xcd + li / 7, cb = li % 7;
    const int rg = w & 3, cg = w >> 2;
    const __amdgpu_buffer_rsrc_t rs = __builtin_amdgcn_make_buffer_rsrc((void*)p.ws, 0, (int)WS_END, 0x00020000);
    STAMP_DECL; STAMP(0);

    {
        const char* xb = p.x + (size_t)(rt * 128 + w * 16) * 896;
#pragma unroll
        for (int j = 0; j < 14; ++j) {
            int i = j * 64 + lane, row = i / 56, pos = i - row * 56;
            int c = (pos & ~7) | ((pos ^ (row >> 1)) & 7);
            glds(xb, row * 896 + c * 16, smb + L_S0 + w * 14336 + j * 1024);
        }
    }
    unsigned qoff[6];
    const char* wq = p.wqkv + (56 * g - 8 * (g & 1));
#pragma unroll
    for (int j = 0; j < 6; ++j) {
        int i = (w * 6 + j) * 64 + lane, row = i / 12, c = i - row * 12;
        qoff[j] = row * 2688 + (c >> 2) * 896 + (c & 3) * 16;
    }
    if (w < 7) {
#pragma unroll
        for (int j = 0; j < 6; ++j) glds(wq, qoff[j], smb + L_R + (w * 6 + j) * 1024);
    }
    const u64 tag = __hip_atomic_load((gu64*)(p.ws + WS_EPOCH), RLX_AGENT) + 1;
    unsigned goff[2];
#pragma unroll
    for (int j = 0; j < 2; ++j) {
        int i = (w * 2 + j) * 64 + lane, row = i / 14, c = i - row * 14;
        goff[j] = row * 3584 + c * 16;
    }
    const char* wgb = p.wg + 224 * g;
    const char* wub = p.wu + 224 * g;
    auto issue_g = [&](int st, int slot, int pc) {
        if (w < 7) glds(((pc & 1) ? wub : wgb) + (size_t)st * 64 * 3584, goff[pc >> 1], smb + slot + (pc & 1) * L_UP + (w * 2 + (pc >> 1)) * 1024);
    };
    const unsigned poff = ((w & 1) * 16 + (lane >> 3)) * 896 + cb * 128 + (((lane & 7) ^ ((lane >> 2) & 4)) * 16);
    auto issue_p = [&](int st, int pc) {
        if (p3) {
            const int kq = w >> 1;
            const int hh = 4 * kq + st - 14;
            const char* b = st < 14 ? p.wd + (size_t)(448 * kq + 32 * st) * 896 : p.wo + (size_t)(28 * hh - 4 * (hh & 1)) * 896;
            glds(b + pc * 8 * 896, poff, smb + pslot(st) + w * 2048 + pc * 1024);
        }
    };
    STAMP(1);
    WAITV(0); BAR();
    STAMP(2);
    bf16x8 xf[28];
    {
        const int row = rg * 32 + (lane & 31);
        lds_ptr xr = sm + L_S0 + row * 896;
        const int sw = (row >> 1) & 7;
#pragma unroll
        for (int s = 0; s < 28; ++s) { int c = 2 * s + hi; xf[s] = *(LAS bf16x8*)(xr + (((c & ~7) | ((c ^ sw) & 7)) * 16)); }
    }
    BAR();
    STAMP(3);
    if (w < 7) {
#pragma unroll
        for (int j = 0; j < 6; ++j) glds(wq + 224 * 2688, qoff[j], smb + L_S0 + (w * 6 + j) * 1024);
    }
#pragma unroll
    for (int j = 0; j < 8; ++j) issue_g(j >> 2, gslot(2 + (j >> 2)), j & 3);
    {
        float s1a[4] = {0.f, 0.f, 0.f, 0.f}, s2a[4] = {0.f, 0.f, 0.f, 0.f};
        const bf16x2 ones = {(__bf16)1.0f, (__bf16)1.0f};
#pragma unroll
        for (int s = 0; s < 28; ++s) {
            u32x4 u = __builtin_bit_cast(u32x4, xf[s]);
#pragma unroll
            for (int e = 0; e < 4; ++e) {
                const unsigned ue = u[e];
                const bf16x2 v = __builtin_bit_cast(bf16x2, ue);
                s1a[e] = __builtin_amdgcn_fdot2_f32_bf16(v, ones, s1a[e], false);
                s2a[e] = __builtin_amdgcn_fdot2_f32_bf16(v, v, s2a[e], false);
            }
        }
        float s1 = (s1a[0] + s1a[1]) + (s1a[2] + s1a[3]), s2 = (s2a[0] + s2a[1]) + (s2a[2] + s2a[3]);
        s1 += xhalf(s1); s2 += xhalf(s2);
        const float mean = s1 * (1.0f / 448.0f);
        const float var = __builtin_fmaxf(s2 * (1.0f / 448.0f) - mean * mean, 0.f);
        const float rstd = __builtin_amdgcn_rsqf(var + 1e-5f), nb = -mean * rstd;
#pragma unroll
        for (int s = 0; s < 28; ++s) {
            u32x4 u = __builtin_bit_cast(u32x4, xf[s]);
#pragma unroll
            for (int e = 0; e < 4; ++e) u[e] = pk2(__builtin_fmaf(bflo(u[e]), rstd, nb), __builtin_fmaf(bfhi(u[e]), rstd, nb));
            xf[s] = __builtin_bit_cast(bf16x8, u);
        }
    }
    const int tile = rt * 4 + rg;
    const int q4 = (lane >> 2) & 3, p4 = lane & 3, g16 = (lane >> 4) & 1;
    f32x16 acc[4];
#pragma unroll
    for (int t = 0; t < 4; ++t) acc[t] = f32x16{};
    {
        const int lo = (8 * hi + q4) * 192 + 32 * g16 + 8 * p4;
        STAMP(4);
        WAITV7(8); RAWBAR();
        STAMP(5);
        lds_ptr qa = sm + lo;
        auto qaddr = [&](int ks) { return qa + (ks < 14 ? L_R + ks * 3072 : L_S0 + (ks - 14) * 3072); };
        if (cg == 0) {
            pipe<28, 4>([&](auto ic) { constexpr int i = ic.v; lds_ptr r = qaddr(i); return trfrag(r, r + 768); },
                        [&](auto ic, bf16x8 a) { constexpr int i = ic.v;
                            acc[0] = MFMA(a, xf[i], acc[0]);
                            if (i == 13) RAWBAR();
                            if (i >= 14 && i < 18) issue_g(2, L_R, i - 14);
                        });
        } else {
            pipe<56, 4>([&](auto ic) { constexpr int i = ic.v; lds_ptr r = qaddr(i >> 1) + 64 + 64 * (i & 1); return trfrag(r, r + 768); },
                        [&](auto ic, bf16x8 a) { constexpr int i = ic.v;
                            if (i & 1) acc[1] = MFMA(xf[i >> 1], a, acc[1]); else acc[0] = MFMA(a, xf[i >> 1], acc[0]);
                            if (i == 27) RAWBAR();
                            if (i >= 28 && i < 32) issue_g(2, L_R, i - 28);
                        });
        }
        STAMP(6);
        const unsigned fo = WS_KV + (tile * 16 + g) * 4096 + lane * 16;
        if (cg == 0) {
            const float qs = 0.1889822365046136f * 1.4426950408889634f;
#pragma unroll
            for (int r = 0; r < 16; ++r) {
                bool pad = (g & 1) ? (r < 4 && hi == 0) : (r >= 12 && hi == 1);
                acc[0][r] = pad ? 0.f : acc[0][r] * qs;
            }
            LAS u32x4* qd = (LAS u32x4*)(sm + L_Q + rg * 2048 + lane * 16);
            qd[0] = pack8(acc[0], 0); qd[64] = pack8(acc[0], 1);
        } else {
            const bool pad = (g & 1) ? ((lane & 31) < 4) : ((lane & 31) >= 28);
#pragma unroll
            for (int r = 0; r < 16; ++r) acc[1][r] = pad ? 1.0f : acc[1][r];
            stws(rs, fo, pack8(acc[0], 0)); stws(rs, fo + 1024, pack8(acc[0], 1));
            stws(rs, fo + 2048, pack8(acc[1], 0)); stws(rs, fo + 3072, pack8(acc[1], 1));
        }
        acc[0] = f32x16{}; acc[1] = f32x16{};
        STAMP(7);
        if (cg == 0) WAITV(4); else WAITV(8);
        BAR();
        STAMP(8);
    }
    u32x4 kvr[6];
    u64 kvp;
    unsigned kvfo;
    { int kt = 4 * rt - 8 + ((6 * w) >> 2) + (lane & 1); kt = kt < 0 ? 0 : kt; kvfo = WS_KVF + (kt * 16 + g) * 64; }
    {
        const int lo = (8 * hi + q4) * 224 + L_UP * g16 + 8 * p4;
        lds_ptr ga = sm + lo + cg * 128;
        auto ghook = [&](int st) {
            if (st == 0) STAMP(9); if (st == 1) STAMP(10); if (st == 3) STAMP(11); if (st == 6) STAMP(12);
            if (st == 0) {
                WAITV7(8);
                if (cg) set_flag(p.ws, WS_KVF + (tile * 16 + g) * 64, tag, lane);
            } else if (st == 1) WAITV(8);
            else if (st == 2) WAITV(8);
            else if (st == 3) { if (p3) WAITV(8); else WAITV(4); kvp = poll_issue(p.ws, kvfo); }
            else if (st == 4) { if (p3) POLL_CHECK(2, kvp, kvfo); else POLL_CHECK(0, kvp, kvfo); }
            RAWBAR();
        };
        auto gdma = [&](int st, int j) {
            if (st == 0 && j < 8) issue_g(3 + (j >> 2), gslot(j >> 2), j & 3);
            if (st == 1 && j < 4) issue_g(5, gslot(2), j);
            if (st == 2 && j < 4) issue_g(6, gslot(3), j);
            if (st == 3 && j < 4) issue_p(j >> 1, j & 1);
            if (st == 4 && j < 2) issue_p(2, j);
            if (st == 5 && j < 4) issue_p(3 + (j >> 1), j & 1);
            if (st == 5 && j >= 4 && j < 10) {
                const int fr = 6 * w + j - 4;
                int kt = 4 * rt - 8 + (fr >> 2); kt = kt < 0 ? 0 : kt;
                kvr[j - 4] = ldws(rs, WS_KV + (kt * 16 + g) * 4096 + (fr & 3) * 1024 + lane * 16);
            }
        };
        auto gaddr = [&](int ks) {
            const int st = ks >> 2;
            const int slot = st == 0 ? gslot(2) : st == 1 ? gslot(3) : st == 2 ? L_R : st == 3 ? gslot(0) : st == 4 ? gslot(1) : st == 5 ? gslot(2) : gslot(3);
            return ga + slot + (ks & 3) * 3584;
        };
        if (cg == 0) {
            pipe<112, 4>([&](auto ic) { constexpr int i = ic.v; lds_ptr r = gaddr(i >> 2) + 32 * (i & 3); return trfrag(r, r + 896); },
                         [&](auto ic, bf16x8 a) { constexpr int i = ic.v; acc[i & 3] = MFMA(a, xf[i >> 2], acc[i & 3]); gdma(i >> 4, i & 15); if ((i & 15) == 15) ghook(i >> 4); });
        } else {
            pipe<84, 4>([&](auto ic) { constexpr int i = ic.v; lds_ptr r = gaddr(i / 3) + 32 * (i % 3); return trfrag(r, r + 896); },
                        [&](auto ic, bf16x8 a) { constexpr int i = ic.v; acc[i % 3] = MFMA(a, xf[i / 3], acc[i % 3]); gdma(i / 12, i % 12); if (i % 12 == 11) ghook(i / 12); });
        }
    }
#pragma unroll
    for (int j = 0; j < 6; ++j) *(LAS u32x4*)(sm + L_KV + (6 * w + j) * 1024 + lane * 16) = kvr[j];
    STAMP(13);
#pragma unroll
    for (int t = 0; t < 4; ++t) {
        if (t < 3 || cg == 0) {
            u32x4 hv;
#pragma unroll
            for (int e = 0; e < 4; ++e) hv[e] = pk2(gelu_mul(acc[t][2 * e], acc[t][8 + 2 * e]), gelu_mul(acc[t][2 * e + 1], acc[t][9 + 2 * e]));
            stws(rs, WS_H + (tile * 112 + 7 * g + 4 * cg + t) * 1024 + lane * 16, hv);
        }
    }
    STAMP(14);
    const int kq = w >> 1, mh = w & 1, tt = rb * 2 + mh;
    const unsigned hfo = WS_HF + ((tt * 16 + 4 * kq) * 2 + (lane & 7)) * 64;
    const unsigned ofo = WS_OF + (tt * 16 + 4 * kq + (lane & 3)) * 64;
    u64 hp = 0;
    u32x4 bf[28], xres;
    {
        BAR();
        const int nt = 5 - cg;
        const int kt0 = cg ? tile - 3 : tile - 8;
        LAS bf16x8* kvl = (LAS bf16x8*)(sm + L_KV + (rg + 5 * cg) * 4096 + lane * 16);
        LAS bf16x8* ql_ = (LAS bf16x8*)(sm + L_Q + rg * 2048 + lane * 16);
        const bf16x8 qf0 = ql_[0], qf1 = ql_[64];
        f32x16 sc[5];
        const int ql = lane & 31;
        float m = -3.0e38f;
#pragma unroll
        for (int j = 0; j < 5; ++j) {
            if (j < nt) {
                f32x16 z = f32x16{};
                z = MFMA(kvl[j * 256], qf0, z);
                z = MFMA(kvl[j * 256 + 64], qf1, z);
                const int kt = kt0 + j;
                if (kt < 0) {
#pragma unroll
                    for (int r = 0; r < 16; ++r) z[r] = -1.0e30f;
                } else if (cg == 0 && j == 0) {
#pragma unroll
                    for (int r = 0; r < 16; ++r) z[r] = ((r & 3) + 8 * (r >> 2) + 4 * hi > ql) ? z[r] : -1.0e30f;
                } else if (cg == 1 && j == 3) {
#pragma unroll
                    for (int r = 0; r < 16; ++r) z[r] = ((r & 3) + 8 * (r >> 2) + 4 * hi <= ql) ? z[r] : -1.0e30f;
                }
#pragma unroll
                for (int r = 0; r < 16; r += 2) m = __builtin_fmaxf(m, __builtin_fmaxf(z[r], z[r + 1]));
                sc[j] = z;
            }
        }
        m = __builtin_fmaxf(m, xhalf(m));
        STAMP(15);
        u32x4 pf[5][2];
#pragma unroll
        for (int j = 0; j < 5; ++j) {
            if (j < nt) {
#pragma unroll
                for (int r = 0; r < 16; ++r) sc[j][r] = __builtin_amdgcn_exp2f(sc[j][r] - m);
                pf[j][0] = pack8(sc[j], 0); pf[j][1] = pack8(sc[j], 1);
            }
        }
        STAMP(16);
        WAITV(0);
        STAMP(17);
        set_flag(p.ws, WS_HF + ((tile * 16 + g) * 2 + cg) * 64, tag, lane);
        if (p3) hp = poll_issue(p.ws, hfo);
        f32x16 o = f32x16{};
#pragma unroll
        for (int j = 0; j < 5; ++j) {
            if (j < nt) {
                o = MFMA(kvl[j * 256 + 128], __builtin_bit_cast(bf16x8, pf[j][0]), o);
                o = MFMA(kvl[j * 256 + 192], __builtin_bit_cast(bf16x8, pf[j][1]), o);
            }
        }
        LAS f32x4* scr = (LAS f32x4*)(sm + L_SCR + rg * 4096 + lane * 16);
        const bool padl = (g & 1) ? (hi == 0) : (hi == 1);
        if (cg == 0) {
            if (g & 1) o[1] = padl ? m : o[1]; else o[13] = padl ? m : o[13];
#pragma unroll
            for (int c = 0; c < 4; ++c) scr[c * 64] = f32x4{o[4 * c], o[4 * c + 1], o[4 * c + 2], o[4 * c + 3]};
        }
        BAR();
        STAMP(18);
        if (p3) {
            POLL_CHECK(0, hp, hfo);
            STAMP(20);
#pragma unroll
            for (int j = 0; j < 8; ++j) issue_p(5 + (j >> 1), j & 1);
            const unsigned hb = WS_H + (tt * 112 + 28 * kq) * 1024 + lane * 16;
#pragma unroll
            for (int i = 0; i < 28; ++i) bf[i] = ldws(rs, hb + i * 1024);
            xres = *(const u32x4*)(p.x + (size_t)(rb * 64 + (tid >> 3)) * 896 + cb * 128 + (tid & 7) * 16);
        }
        if (cg == 1) {
            f32x16 oa;
#pragma unroll
            for (int c = 0; c < 4; ++c) { f32x4 v = scr[c * 64]; oa[4 * c] = v[0]; oa[4 * c + 1] = v[1]; oa[4 * c + 2] = v[2]; oa[4 * c + 3] = v[3]; }
            float ma = (g & 1) ? oa[1] : oa[13], la = (g & 1) ? oa[0] : oa[12], lb = (g & 1) ? o[0] : o[12];
            float ma2 = xhalf(ma), la2 = xhalf(la), lb2 = xhalf(lb);
            ma = padl ? ma : ma2; la = padl ? la : la2; lb = padl ? lb : lb2;
            const float mm = __builtin_fmaxf(ma, m);
            float wa = __builtin_amdgcn_exp2f(ma - mm), wb = __builtin_amdgcn_exp2f(m - mm);
            const float inv = __builtin_amdgcn_rcpf(la * wa + lb * wb);
            wa *= inv; wb *= inv;
#pragma unroll
            for (int r = 0; r < 16; ++r) {
                bool pad = (g & 1) ? (r < 4 && hi == 0) : (r >= 12 && hi == 1);
                o[r] = pad ? 0.f : oa[r] * wa + o[r] * wb;
            }
            const unsigned fo = WS_O + (tile * 16 + g) * 2048 + lane * 16;
            stws(rs, fo, pack8(o, 0)); stws(rs, fo + 1024, pack8(o, 1));
            if (!p3) { WAITV(0); set_flag(p.ws, WS_OF + (tile * 16 + g) * 64, tag, lane); }
        }
    }
    STAMP(19);
    if (!p3) {
        if (blockIdx.x == 255 && w == 0) {
#pragma unroll
            for (int j = 0; j < 4; ++j) { int id = j * 64 + lane; wait_flags(p.ws, WS_KVF + ((id >> 4) * 64 + (id & 15)) * 64, tag); }
            set_flag(p.ws, WS_EPOCH, tag, lane);
        }
        STAMP_OUT;
        return;
    }
    {
        const unsigned ob = WS_O + (tt * 16 + 4 * kq) * 2048 + lane * 16;
        u64 op = 0;
        f32x16 af[2], as[2];
        af[0] = f32x16{}; af[1] = f32x16{}; as[0] = f32x16{}; as[1] = f32x16{};
        const int lo = kq * 4096 + (4 * hi + q4) * 128 + 32 * g16 + 8 * p4;
        const int n0 = ((q4 >> 1) & 1) * 64;
        lds_ptr pa = sm + lo + n0, pb = sm + lo + (64 - n0);
        auto phook = [&](int st) {
            if (st == 0) STAMP(21); if (st == 6) STAMP(22); if (st == 8) STAMP(23); if (st == 13) STAMP(24); if (st == 17) STAMP(25);
            if (st == 2 && cg) { WAITV(6); set_flag(p.ws, WS_OF + (tile * 16 + g) * 64, tag, lane); }
            if (st == 5) op = poll_issue(p.ws, ofo);
            if (st == 7) POLL_CHECK(4, op, ofo);
            else if (st == 8) WAITV(22); else if (st == 9) WAITV(20); else if (st == 10) WAITV(18); else if (st == 11) WAITV(16);
            else if (st == 12) WAITV(14); else if (st == 13) WAITV(12); else if (st == 14) WAITV(10); else if (st == 15) WAITV(8);
            if (st < 16) RAWBAR();
        };
        pipe<72, 5>([&](auto ic) { constexpr int i = ic.v; lds_ptr r = ((i & 1) ? pb : pa) + pslot(i >> 2) + ((i >> 1) & 1) * 2048; return trfrag(r, r + 1024); },
                    [&](auto ic, bf16x8 a) { constexpr int i = ic.v;
                        constexpr int ks = i >> 1, st = i >> 2, j = i & 3;
                        const bf16x8 b = __builtin_bit_cast(bf16x8, bf[ks % 28]);
                        if (ks < 28) af[i & 1] = MFMA(a, b, af[i & 1]); else as[i & 1] = MFMA(a, b, as[i & 1]);
                        if (st == 1) issue_p(9 + (j >> 1), j & 1);
                        if (st >= 2 && st <= 8 && j < 2) issue_p(st + 9, j);
                        if (st == 8 && j >= 2) {
#pragma unroll
                            for (int e = 0; e < 4; ++e) bf[(j - 2) * 4 + e] = ldws(rs, ob + ((j - 2) * 4 + e) * 1024);
                        }
                        if (j == 3) phook(st);
                    });
        BAR();
        {
            lds_ptr d = sm + kq * 17408 + (mh * 32 + (lane & 31)) * 272 + hi * 16;
#pragma unroll
            for (int nh = 0; nh < 2; ++nh)
#pragma unroll
                for (int c = 0; c < 4; ++c) {
                    *(LAS f32x4*)(d + nh * 128 + c * 32) = f32x4{af[nh][4 * c], af[nh][4 * c + 1], af[nh][4 * c + 2], af[nh][4 * c + 3]};
                    *(LAS f32x4*)(d + 69632 + nh * 128 + c * 32) = f32x4{as[nh][4 * c], as[nh][4 * c + 1], as[nh][4 * c + 2], as[nh][4 * c + 3]};
                }
        }
        BAR();
        {
            lds_ptr d = sm + (tid >> 3) * 272 + (tid & 7) * 32;
            f32x4 f0 = {}, f1 = {}, s0 = {}, s1 = {};
#pragma unroll
            for (int k = 0; k < 4; ++k) {
                f0 += *(LAS f32x4*)(d + k * 17408); f1 += *(LAS f32x4*)(d + k * 17408 + 16);
                s0 += *(LAS f32x4*)(d + 69632 + k * 17408); s1 += *(LAS f32x4*)(d + 69632 + k * 17408 + 16);
            }
            float r[8];
#pragma unroll
            for (int e = 0; e < 8; ++e) {
                const float f = e < 4 ? f0[e & 3] : f1[e & 3], s = e < 4 ? s0[e & 3] : s1[e & 3];
                const float gt = f * __builtin_amdgcn_rcpf(1.0f + __builtin_amdgcn_exp2f(-1.4426950408889634f * f));
                const float xv = (e & 1) ? bfhi(xres[e >> 1]) : bflo(xres[e >> 1]);
                r[e] = __builtin_fmaf(s, gt, xv);
            }
            u32x4 ov = {pk2(r[0], r[1]), pk2(r[2], r[3]), pk2(r[4], r[5]), pk2(r[6], r[7])};
            *(u32x4*)(p.out + (size_t)(rb * 64 + (tid >> 3)) * 896 + cb * 128 + (tid & 7) * 16) = ov;
            STAMP(26); STAMP_OUT;
        }
    }
}

extern "C" void kernel_launch(void* const* d_in, const int* in_sizes, int n_in, void* d_out, int out_size, void* d_ws, size_t ws_size,
                              hipStream_t stream) {
    (void)in_sizes; (void)n_in; (void)out_size; (void)ws_size;
    Params p;
    p.x = (const char*)d_in[0]; p.wqkv = (const char*)d_in[1]; p.wo = (const char*)d_in[2];
    p.wg = (const char*)d_in[4]; p.wu = (const char*)d_in[5]; p.wd = (const char*)d_in[6];
    p.out = (char*)d_out; p.ws = (char*)d_ws;
    (void)hipFuncSetAttribute((const void*)fused_kernel, hipFuncAttributeMaxDynamicSharedMemorySize, LDS_BYTES);
    fused_kernel<<<256, 512, LDS_BYTES, stream>>>(p);
}
```

```cpp
#include <hip/hip_runtime.h>
#include <hip/hip_bf16.h>

typedef __attribute__((ext_vector_type(8))) __bf16 bf16x8;
typedef __attribute__((ext_vector_type(2))) __bf16 bf16x2;
typedef __attribute__((ext_vector_type(16))) float f32x16;
typedef __attribute__((ext_vector_type(4))) float f32x4;
typedef __attribute__((ext_vector_type(2))) float f32x2;
typedef __attribute__((ext_vector_type(4))) unsigned u32x4;
typedef __attribute__((ext_vector_type(2))) unsigned u32x2;
typedef short v4i16_t __attribute__((ext_vector_type(4)));
typedef short v8i16_t __attribute__((ext_vector_type(8)));
typedef unsigned long long u64;
#define LAS __attribute__((address_space(3)))
typedef LAS char* lds_ptr;
typedef __attribute__((address_space(1))) u64 gu64;
#define DI __device__ __forceinline__
#define MFMA(a, b, c) __builtin_amdgcn_mfma_f32_32x32x16_bf16(a, b, c, 0, 0, 0)
#define RLX_AGENT __ATOMIC_RELAXED, __HIP_MEMORY_SCOPE_AGENT
#define WAITV(N) asm volatile("s_waitcnt vmcnt(" #N ")" ::: "memory")
#define BAR() asm volatile("s_waitcnt lgkmcnt(0)\n\ts_barrier" ::: "memory")
template <int N> DI void waitv() { asm volatile("s_waitcnt vmcnt(%0)" :: "i"(N) : "memory"); }

#define STAMPS 0

constexpr int AUX_SC1 = 16;
constexpr unsigned WS_EPOCH = 0;
constexpr unsigned WS_KVF = 4096;
constexpr unsigned WS_OF = WS_KVF + 65536;
constexpr unsigned WS_HF = WS_OF + 65536;
constexpr unsigned WS_CS = WS_HF + 131072;
constexpr unsigned WS_STAMP = 512 * 1024;
constexpr unsigned WS_KV = 1u << 20,
                    WS_O = 7u << 20, WS_H = 9u << 20, WS_END = 16u << 20;
constexpr int L_R = 0, L_S0 = 48640, L_GS = 28800, L_UP = 14336 + 128;
constexpr int L_Q = 32768, L_V = 0, L_K = 49152;
constexpr int L_CS_BIG = 43008, L_ST = 46592, L_RS = 41984;
constexpr int LDS_BYTES = 163840;
constexpr int CODE_BYTES = 49152;
DI int kvaddr(int fr) { return ((fr & 2) ? L_V : L_K) + (fr >> 2) * 2048 + (fr & 1) * 1024; }
DI constexpr int gslot(int i) { return L_S0 + i * L_GS; }
DI constexpr int pslot(int s) { return (s % 10 < 5 ? s % 10 + 5 : s % 10 < 7 ? s % 10 - 2 : s % 10 - 7) * 16384; }

struct Params {
    const char* x; const char* wqkv; const char* wo; const char* wg; const char* wu; const char* wd;
    char* out; char* ws;
};

DI void glds(const char* sbase, unsigned voff, unsigned lds) {
    unsigned keep;
    asm volatile("s_mov_b32 %0, m0\n\ts_mov_b32 m0, %3\n\ts_nop 0\n\tglobal_load_lds_dwordx4 %1, %2\n\ts_mov_b32 m0, %0"
                 : "=&s"(keep) : "v"(voff), "s"(sbase), "s"(lds) : "memory");
}
DI void touch(const char* sbase, unsigned voff, unsigned lds) {
    unsigned keep;
    asm volatile("s_mov_b32 %0, m0\n\ts_mov_b32 m0, %3\n\ts_nop 0\n\tglobal_load_lds_dword %1, %2\n\ts_mov_b32 m0, %0"
                 : "=&s"(keep) : "v"(voff), "s"(sbase), "s"(lds) : "memory");
}
DI v4i16_t trr(lds_ptr p) { return __builtin_amdgcn_ds_read_tr16_b64_v4i16((LAS v4i16_t*)p); }
DI bf16x8 trfrag(lds_ptr p0, lds_ptr p1) {
    v4i16_t a = trr(p0), b = trr(p1);
    return __builtin_bit_cast(bf16x8, __builtin_shufflevector(a, b, 0, 1, 2, 3, 4, 5, 6, 7));
}
DI unsigned pk2(float a, float b) { f32x2 v = {a, b}; return __builtin_bit_cast(unsigned, __builtin_convertvector(v, bf16x2)); }
DI u32x4 pack8(const f32x16& c, int s) {
    u32x4 r = {pk2(c[8 * s], c[8 * s + 1]), pk2(c[8 * s + 2], c[8 * s + 3]), pk2(c[8 * s + 4], c[8 * s + 5]), pk2(c[8 * s + 6], c[8 * s + 7])};
    return r;
}
DI float bflo(unsigned u) { return __uint_as_float(u << 16); }
DI float bfhi(unsigned u) { return __uint_as_float(u & 0xffff0000u); }
DI float rbf(float a) { return bflo(pk2(a, a) & 0xffffu); }
DI float max3(float a, float b, float c) {
#pragma clang fp reassociate(on)
    return __builtin_amdgcn_fmed3f(__builtin_amdgcn_fmed3f(a, b, __builtin_inff()), c, __builtin_inff());
}
DI float xhalf(float v) {
    auto r = __builtin_amdgcn_permlane32_swap(__float_as_uint(v), __float_as_uint(v), false, false);
    return __uint_as_float((threadIdx.x & 32) ? r[0] : r[1]);
}
DI u32x4 ldws(__amdgpu_buffer_rsrc_t rs, unsigned off) { return __builtin_amdgcn_raw_buffer_load_b128(rs, (int)off, 0, AUX_SC1); }
DI void stws(__amdgpu_buffer_rsrc_t rs, unsigned off, u32x4 v) { __builtin_amdgcn_raw_buffer_store_b128(v, rs, (int)off, 0, AUX_SC1); }
DI void set_flag(char* ws, unsigned off, u64 tag, int lane) {
    (void)lane;
    __hip_atomic_store((gu64*)(ws + off), tag, RLX_AGENT);
}
DI void wait_flags(char* ws, unsigned off, u64 tag) {
    for (int spin = 0; spin < (1 << 20); ++spin) {
        u64 v = __hip_atomic_load((gu64*)(ws + off), RLX_AGENT);
        if (__all(v == tag)) break;
        __builtin_amdgcn_s_sleep(2);
    }
    __builtin_amdgcn_fence(__ATOMIC_ACQUIRE, "wavefront");
}
DI u64 poll_issue(char* ws, unsigned off) {
    u64 v;
    asm volatile("global_load_dwordx2 %0, %1, %2 sc1" : "=v"(v) : "v"(off), "s"(ws) : "memory");
    return v;
}
#define WAIT_SEL(c, NA, ...) asm volatile("s_cmp_lg_u32 %[sel], 0\n\ts_cbranch_scc1 1f\n\ts_waitcnt vmcnt(0)\n1:\n\ts_waitcnt vmcnt(" #NA ")" \
                                          : __VA_ARGS__ : [sel] "s"(c) : "scc", "memory")
#define POLL_CHECK(c, N, v, off) do { WAIT_SEL(c, N, "+v"(v)); if (!__all(v == tag)) wait_flags(p.ws, off, tag); } while (0)
DI u32x4 gran_issue(char* ws, unsigned off) {
    u32x4 v;
    asm volatile("global_load_dwordx4 %0, %1, %2 sc1" : "=v"(v) : "v"(off), "s"(ws) : "memory");
    return v;
}
DI bool gran_ok(u32x4 v, u64 tag) { return v[1] == (unsigned)tag && v[3] == (unsigned)(tag >> 32); }
DI unsigned gran_spin(__amdgpu_buffer_rsrc_t rs, unsigned off, u64 tag) {
    u32x4 v = ldws(rs, off);
    for (int spin = 0; spin < (1 << 20) && !__all(gran_ok(v, tag)); ++spin) { __builtin_amdgcn_s_sleep(2); v = ldws(rs, off); }
    return v[0];
}
#define STAMP(i)
#define STAMP_DECL
#define STAMP_OUT

#define SBAR() __builtin_amdgcn_sched_barrier(0)
#define RAWBAR() asm volatile("s_barrier" ::: "memory")
template <int V> struct IC { static constexpr int v = V; constexpr operator int() const { return V; } };
template <int I, int N, class F>
DI void sfor(F&& f) { if constexpr (I < N) { f(IC<I>{}); sfor<I + 1, N>(f); } }
template <int N, int D, class LD, class MM>
DI void pipe(LD ld, MM mm) {
    bf16x8 buf[D];
    sfor<0, D - 1>([&](auto i) { buf[i.v] = ld(i); });
    SBAR();
    sfor<0, N>([&](auto i) {
        if constexpr (i.v + D - 1 < N) buf[(i.v + D - 1) % D] = ld(IC<i.v + D - 1>{});
        SBAR();
        mm(i, buf[i.v % D]);
        SBAR();
    });
}
struct LB {
    unsigned b[3];
    DI LB(unsigned a) { b[0] = a; b[1] = a + 0xc000; b[2] = a + 0x18000 + 0x2000; asm volatile("" : "+v"(b[0]), "+v"(b[1]), "+v"(b[2])); }
    DI lds_ptr at(int off) const { return off < 0xc000 ? (lds_ptr)(size_t)b[0] + off : off < 0x1a000 ? (lds_ptr)(size_t)b[1] + (off - 0xc000) : (lds_ptr)(size_t)b[2] + (off - 0x1a000); }
};
DI float gelu_mul(float a, float b, float c1, float c2, float ir2) {
    float e = __builtin_amdgcn_exp2f(a * __builtin_fmaf(a * a, c2, c1));
    return a * b * __builtin_amdgcn_rcpf(__builtin_fmaf(e, ir2, ir2));
}

__global__ __launch_bounds__(512, 2) void fused_kernel(Params p) {
    extern __shared__ __attribute__((aligned(16))) char smem_[];
    lds_ptr sm = (lds_ptr)smem_;
    const unsigned smb = (unsigned)(size_t)sm;
    const int tid = threadIdx.x, lane = tid & 63;
    const int w = __builtin_amdgcn_readfirstlane(tid >> 6);
    const int hi = lane >> 5;
    const int xcd = blockIdx.x & 7, li = blockIdx.x >> 3;
    const int rt = 4 * (xcd >> 1) + (li >> 3), g = 8 * (xcd & 1) + (li & 7);
    const bool p3 = li < 28;
    const int rb = 4 * xcd + li / 7, cb = li % 7;
    const int rg = w & 3, cg = w >> 2;
    const int p3i = 28 - li > 0 ? 28 - li : 0;
    const __amdgpu_buffer_rsrc_t rs = __builtin_amdgcn_make_buffer_rsrc((void*)p.ws, 0, (int)WS_END, 0x00020000);
    STAMP_DECL; STAMP(0);

    const char* wq = p.wqkv + (56 * g - 8 * (g & 1));
    const char* wgb = p.wg + 224 * g;
    const char* wub = p.wu + 224 * g;
    {
        const char* xb = p.x + (size_t)(rt * 128 + w * 16) * 896;
        const unsigned xo = (lane >> 3) * 896 + ((lane & 7) ^ (lane >> 4)) * 16;
#pragma unroll
        for (int j = 0; j < 14; ++j)
            glds(xb + (j / 7) * 8 * 896 + (j % 7) * 128, j < 7 ? xo : xo ^ 64, smb + L_S0 + w * 14336 + j * 1024);
    }
    const unsigned qo = (lane >> 2) * 2688 + (lane & 3) * 16;
    auto issue_q = [&](int half, int pc) {
        if (w < 7) glds(wq + (pc >> 1) * 896 + (size_t)(half * 224 + (w * 2 + (pc & 1)) * 16) * 2688, qo,
                        smb + (half ? L_S0 : L_R) + (pc >> 1) * 14336 + (w * 2 + (pc & 1)) * 1024);
    };
#pragma unroll
    for (int j = 0; j < 6; ++j) issue_q(0, j);
    u32x4 cw[4];
    if (rt < 3 && w < 7) {
#pragma unroll
        for (int j = 0; j < 4; ++j) cw[j] = *(const u32x4*)(wq + rt * 896 + (size_t)(w * 64 + j * 16) * 2688 + qo);
    }
    const u64 tag = __hip_atomic_load((gu64*)(p.ws + WS_EPOCH), RLX_AGENT) + 1;
    const int codew = li == 31 && w < CODE_BYTES / 8192;
    if (codew) {
        const char* pc;
        asm volatile("s_getpc_b64 %0" : "=s"(pc));
        touch(pc + w * 8192, lane * 128, smb + L_CS_BIG + w * 512);
    }
    unsigned goff[4];
    const int gp0 = cg ? 12 + rg * 4 : rg * 3;
#pragma unroll
    for (int j = 0; j < 4; ++j) {
        const int pc = gp0 + j, i = (pc % 14) * 64 + lane, row = i / 14, c = i - row * 14;
        goff[j] = row * 3584 + c * 16;
    }
    auto issue_g = [&](int st, int slot, int n) {
        const int pc = gp0 + n;
        if (n < 3 || cg) glds((pc >= 14 ? wub : wgb) + (size_t)st * 64 * 3584, goff[n], smb + slot + (pc >= 14 ? L_UP - 14336 : 0) + pc * 1024);
    };
    const unsigned poff = ((w & 1) * 16 + (lane >> 3)) * 896 + cb * 128 + (((lane & 7) ^ ((lane >> 2) & 4)) * 16);
    auto issue_p = [&](int st, int pc) {
        if (p3) {
            const int kq = w >> 1;
            const int hh = 4 * kq + st - 14;
            const char* b = st < 14 ? p.wd + (size_t)(448 * kq + 32 * st) * 896 : p.wo + (size_t)(28 * hh - 4 * (hh & 1)) * 896;
            glds(b + pc * 8 * 896, poff, smb + pslot(st) + w * 2048 + pc * 1024);
        }
    };
    auto cs_partial = [&]() {
        float cs[8];
#pragma unroll
        for (int e = 0; e < 4; ++e) {
            cs[2 * e] = (bflo(cw[0][e]) + bflo(cw[1][e])) + (bflo(cw[2][e]) + bflo(cw[3][e]));
            cs[2 * e + 1] = (bfhi(cw[0][e]) + bfhi(cw[1][e])) + (bfhi(cw[2][e]) + bfhi(cw[3][e]));
        }
#pragma unroll
        for (int e = 0; e < 8; ++e) {
            cs[e] += __builtin_bit_cast(float, __builtin_amdgcn_update_dpp(0, __builtin_bit_cast(int, cs[e]), 0x124, 0xf, 0xf, false));
            cs[e] += __builtin_bit_cast(float, __builtin_amdgcn_update_dpp(0, __builtin_bit_cast(int, cs[e]), 0x128, 0xf, 0xf, false));
        }
        {
            LAS f32x4* d = (LAS f32x4*)(sm + L_CS_BIG + w * 512 + (lane >> 4) * 128 + (lane & 3) * 32);
            d[0] = f32x4{cs[0], cs[1], cs[2], cs[3]}; d[1] = f32x4{cs[4], cs[5], cs[6], cs[7]};
        }
    };
    auto cs_publish = [&]() {
        float v = 0.f;
        LAS float* d = (LAS float*)(sm + L_CS_BIG + (lane & 31) * 4 + hi * 1792);
#pragma unroll
        for (int j = 0; j < 14; ++j) v += d[j * 32];
        v += xhalf(v);
        const unsigned h_ = pk2(v, v) & 0xffffu;
        const unsigned pv = h_ | (pk2(v - bflo(h_), 0.f) << 16);
        stws(rs, WS_CS + ((g * 10 + rt) * 32 + (lane & 31)) * 16, u32x4{pv, (unsigned)tag, pv, (unsigned)(tag >> 32)});
    };
    if (rt < 3 && w < 7) cs_partial();
    STAMP(1);
    if (codew) WAITV(1); else WAITV(0);
    BAR();
    STAMP(2);
    if (rt < 3 && w == 7) cs_publish();
    const int tile = rt * 4 + rg;
    const int q4 = (lane >> 2) & 3, p4 = lane & 3, g16 = (lane >> 4) & 1;
    const int kq = w >> 1, mh = w & 1, tt = p3 ? rb * 2 + mh : 0;
    const unsigned hfo = WS_HF + ((tt * 16 + 4 * kq) * 2 + (lane & 7)) * 64;
    const unsigned hb = WS_H + (tt * 112 + 28 * kq) * 1024 + lane * 16;
    u32x4 bf[28];
    auto phase1 = [&](auto cgc) {
    constexpr int cg = cgc.v;
    constexpr int NG = 3 + cg;
    auto issue_gc = [&](int st, int slot, int n) { if (n < NG) issue_g(st, slot, n); };
    bf16x8 xf[28];
    {
        const int t = lane & 31;
        lds_ptr xr = sm + L_S0 + (rg * 4 + (t >> 3)) * 7168 + (t & 7) * 128;
        const int sw = (t >> 1) & 7;
#pragma unroll
        for (int s = 0; s < 28; ++s) { int c = 2 * s + hi; xf[s] = *(LAS bf16x8*)(xr + (c >> 3) * 1024 + (((c ^ sw) & 7) * 16)); }
    }
    BAR();
    STAMP(3);
    const bool csd = rt >= 3 && rt < 10, csw = csd && w < 7;
    {
        float s1a[4] = {0.f, 0.f, 0.f, 0.f}, s2a[4] = {0.f, 0.f, 0.f, 0.f};
        const bf16x2 ones = {(__bf16)1.0f, (__bf16)1.0f};
        auto stat = [&](const bf16x8& xv) {
            u32x4 u = __builtin_bit_cast(u32x4, xv);
#pragma unroll
            for (int e = 0; e < 4; ++e) {
                const unsigned ue = u[e];
                const bf16x2 v = __builtin_bit_cast(bf16x2, ue);
                s1a[e] = __builtin_amdgcn_fdot2_f32_bf16(v, ones, s1a[e], false);
                s2a[e] = __builtin_amdgcn_fdot2_f32_bf16(v, v, s2a[e], false);
            }
        };
        if (cg == 0) {
#pragma unroll
            for (int s = 0; s < 14; ++s) { stat(xf[s]); if (s % 2 == 0 && s < 12) issue_q(1, s / 2); }
        } else {
#pragma unroll
            for (int s = 14; s < 28; ++s) { stat(xf[s]); if (s % 2 == 1 && s < 26) issue_q(1, (s - 14) / 2); }
        }
        f32x2 sv = {(s1a[0] + s1a[1]) + (s1a[2] + s1a[3]), (s2a[0] + s2a[1]) + (s2a[2] + s2a[3])};
        sv[0] += xhalf(sv[0]); sv[1] += xhalf(sv[1]);
        *(LAS f32x2*)(sm + L_ST + w * 256 + (lane & 31) * 8) = sv;
    }
    f32x16 acc[4];
    const float keeplo = ((g & 1) && hi == 0) ? 0.f : 1.f, keephi = (!(g & 1) && hi == 1) ? 0.f : 1.f;
    float rstd, ir2;
    bf16x8 mfr;
#pragma unroll
    for (int t = 0; t < 4; ++t) acc[t] = f32x16{};
    {
        STAMP(4);
        const unsigned cso = WS_CS + (g * 10 * 32 + (lane & 31)) * 16;
        u32x4 cg0v, cg1v;
        const LB qa(smb + (8 * hi + q4) * 64 + 32 * g16 + 8 * p4 + cg * 14336);
        auto qops = [&](int half, int n) {
            if (half == 0 && n >= 5 && n < 13) issue_gc((n - 5) >> 2, gslot(2 + ((n - 5) >> 2)), (n - 5) & 3);
            if (half == 1 && n == 2) { cg0v = gran_issue(p.ws, cso + cg * 512); cg1v = gran_issue(p.ws, cso + 1024); }
            if (half == 1 && n >= 4 && n < 12 && !(n & 1)) issue_gc(2, L_R, (n - 4) >> 1);
        };
        sfor<0, 2>([&](auto hc) {
            constexpr int half = hc.v, base = half ? L_S0 : L_R;
            pipe<28, 5>([&](auto ic) { constexpr int i = ic.v;
                            bf16x8 a;
                            if ((i & 1) == 0 || cg) { lds_ptr r = qa.at(base + (i >> 1) * 1024 + (i & 1) * 14336); a = trfrag(r, r + 256); } else asm("" : "=v"(a));
                            return a;
                        },
                        [&](auto ic, bf16x8 a) { constexpr int i = ic.v;
                            if (i & 1) { if (cg) acc[1] = MFMA(xf[half * 14 + (i >> 1)], a, acc[1]); }
                            else { acc[0] = MFMA(a, xf[half * 14 + (i >> 1)], acc[0]); qops(half, i >> 1); }
                        });
            if (half == 0) {
                waitv<2 * NG>();
                BAR();
                STAMP(5);
            }
        });
        STAMP(6);
        {
            const f32x2 sa = *(LAS f32x2*)(sm + L_ST + rg * 256 + (lane & 31) * 8), sb = *(LAS f32x2*)(sm + L_ST + (rg + 4) * 256 + (lane & 31) * 8);
            const float mean = (sa[0] + sb[0]) * (1.0f / 448.0f);
            ir2 = __builtin_fmaxf((sa[1] + sb[1]) * (1.0f / 448.0f) - mean * mean, 0.f) + 1e-5f;
            rstd = __builtin_amdgcn_rsqf(ir2);
            const float mh_ = -0.5f * mean;
            const unsigned m0_ = pk2(mh_, mh_), m1_ = pk2(mh_ - bflo(m0_ & 0xffffu), mh_ - bflo(m0_ & 0xffffu));
            mfr = __builtin_bit_cast(bf16x8, u32x4{m0_, m1_, 0u, 0u});
        }
        asm volatile("s_waitcnt vmcnt(%2)" : "+v"(cg0v), "+v"(cg1v) : "i"(NG) : "memory");
        RAWBAR();
        {
            unsigned c0 = cg0v[0], c1 = cg1v[0];
            if (!__all(gran_ok(cg0v, tag) && gran_ok(cg1v, tag))) { c0 = gran_spin(rs, cso + cg * 512, tag); c1 = gran_spin(rs, cso + 1024, tag); }
            acc[0] = MFMA(__builtin_bit_cast(bf16x8, u32x4{c0, c0, 0u, 0u}), mfr, acc[0]);
            if (cg) acc[1] = MFMA(mfr, __builtin_bit_cast(bf16x8, u32x4{c1, c1, 0u, 0u}), acc[1]);
        }
        const unsigned fo = WS_KV + (tile * 16 + g) * 4096 + lane * 16;
        if (cg == 0) {
            const float qs = 0.1889822365046136f * 1.4426950408889634f * rstd;
            const float qlo = qs * keeplo, qhi = qs * keephi;
#pragma unroll
            for (int r = 0; r < 16; ++r) acc[0][r] *= r < 4 ? qlo : r >= 12 ? qhi : qs;
            LAS u32x4* qd = (LAS u32x4*)(sm + L_Q + rg * 2048 + lane * 16);
            qd[0] = pack8(acc[0], 0); qd[64] = pack8(acc[0], 1);
        } else {
            const float vpad = ((g & 1) ? ((lane & 31) < 4) : ((lane & 31) >= 28)) ? 1.f : 0.f, vkeep = 1.f - vpad;
            LAS float* rt_ = (LAS float*)(sm + L_RS + rg * 128);
            rt_[lane & 31] = rstd;
#pragma unroll
            for (int c = 0; c < 4; ++c) {
                const f32x4 rr = *(LAS f32x4*)(rt_ + 8 * c + 4 * hi);
#pragma unroll
                for (int e = 0; e < 4; ++e) { acc[1][4 * c + e] = __builtin_fmaf(acc[1][4 * c + e] * vkeep, rr[e], vpad); acc[0][4 * c + e] *= rstd; }
            }
            stws(rs, fo, pack8(acc[0], 0)); stws(rs, fo + 1024, pack8(acc[0], 1));
            stws(rs, fo + 2048, pack8(acc[1], 0)); stws(rs, fo + 3072, pack8(acc[1], 1));
        }
        acc[0] = f32x16{}; acc[1] = f32x16{};
        STAMP(7);

        STAMP(8);
    }
    u32x4 kvr[6], csg[4];
    const unsigned csgo = WS_CS + ((g * 10 + 3 + 4 * cg) * 32 + (lane & 31)) * 16;
    const char* csb = ((lane & 2) ? wub : wgb) + (rt - 3) * 32 + (lane & 1) * 16 + (size_t)(w * 64 + (lane >> 2)) * 3584;
    u64 kvp;
    unsigned kvfo;
    { int kt = 4 * rt - 8 + ((6 * w) >> 2) + (lane & 1); kt = kt < 0 ? 0 : kt; kvfo = WS_KVF + (kt * 16 + g) * 64; }
    f32x16 o;
    float m;
    LAS f32x4* scr = (LAS f32x4*)(sm + (rg < 2 ? 24576 : 65536) + rg * 4096 + lane * 16);
    const bool padl = (g & 1) ? (hi == 0) : (hi == 1);
    {
        const LB ga(smb + (8 * hi + q4) * 224 + L_UP * g16 + 8 * p4 + cg * 128);
        auto ghook = [&](int st) {
            if (st == 0) STAMP(9); if (st == 1) STAMP(10); if (st == 4) STAMP(11); if (st == 6) STAMP(12);
            if (st == 0) {
                if (csw) waitv<NG + 2>(); else waitv<NG>();
                if (cg) set_flag(p.ws, WS_KVF + (tile * 16 + g) * 64, tag, lane);
            } else if (st == 1) { if (csw) waitv<NG + 2>(); else waitv<NG>(); }
            else if (st == 2) {
                waitv<NG>();
                if (csw) { asm volatile("" : "+v"(cw[0]), "+v"(cw[1]), "+v"(cw[2]), "+v"(cw[3])); cs_partial(); }
                kvp = poll_issue(p.ws, kvfo);
                BAR();
                if (csd && w == 7) cs_publish();
                return;
            } else if (st == 3) { asm volatile("s_waitcnt vmcnt(%1)" : "+v"(kvp) : "i"(NG) : "memory"); if (!__all(kvp == tag)) wait_flags(p.ws, kvfo, tag); }
            else if (st == 4) WAITV(6);
            else if (st == 5) asm volatile("s_waitcnt lgkmcnt(0)" ::: "memory");
            RAWBAR();
        };
        auto gdma = [&](int st, int n) {
            if (st < 4) issue_gc(3 + st, gslot(st), n);
            if (st < 2 && n < 2 && csw)
                asm volatile("global_load_dwordx4 %0, %1, off" : "=v"(cw[2 * st + n]) : "v"(csb + (size_t)(2 * st + n) * 16 * 3584) : "memory");
            if (st == 4 && n < 3) {
#pragma unroll
                for (int e = 0; e < 2; ++e) {
                    const int fr = 6 * w + 2 * n + e;
                    int kt = 4 * rt - 8 + (fr >> 2); kt = kt < 0 ? 0 : kt;
                    kvr[2 * n + e] = ldws(rs, WS_KV + (kt * 16 + g) * 4096 + (fr & 3) * 1024 + lane * 16);
                }
            }
            if (st == 5 && n == 1) {
#pragma unroll
                for (int e = 0; e < 6; ++e) *(LAS u32x4*)(sm + kvaddr(6 * w + e) + lane * 16) = kvr[e];
            }
            if (st == 5 && n == 2) {
                const int wid = li * 8 + w;
                if (wid < 245) touch(wid < 196 ? p.wd + wid * 8192 : p.wo + (wid - 196) * 8192, lane * 128, smb + 40960);
            }
            if (st == 5 && n == 3) {
#pragma unroll
                for (int t = 0; t < 4 - cg; ++t) csg[t] = gran_issue(p.ws, csgo + t * 512);
            }
            if (st == 6 && n < 3) { issue_p(n, 0); issue_p(n, 1); }
        };
        f32x16 sc[5];
        bf16x8 qf0, qf1, fa, fb;
        auto side = [&](auto kc) {
            constexpr int k = kc.v;
            auto kv = [&](int j, int part) { return *(LAS bf16x8*)(sm + kvaddr(4 * (rg + 5 * cg + (j < 4 ? j : 4 - cg)) + part) + lane * 16); };
            if constexpr (k == 0) {
                LAS bf16x8* ql_ = (LAS bf16x8*)(sm + L_Q + rg * 2048 + lane * 16);
                qf0 = ql_[0]; qf1 = ql_[64]; fa = kv(0, 0); fb = kv(0, 1); m = -3.0e38f;
            } else if constexpr (k <= 5) {
                constexpr int j = k - 1;
                const bf16x8 ka = fa, kb = fb;
                if constexpr (j < 4) { fa = kv(j + 1, 0); fb = kv(j + 1, 1); } else fa = kv(0, 2);
                f32x16 z = f32x16{};
                if constexpr (j == 0 || j == 3) {
                    const int ql = lane & 31;
                    const float big = ((j == 0) == (cg == 0)) ? 0x1p100f : 0.f;
                    const float b0 = (float)(j ? ql - 4 * hi : 4 * hi - ql - 1) * big;
#pragma unroll
                    for (int r = 0; r < 16; ++r) z[r] = __builtin_fminf(__builtin_fmaf(j ? -big : big, (float)((r & 3) + 8 * (r >> 2)), b0), 0.f);
                }
                if (j < 4 || cg == 0) { z = MFMA(ka, qf0, z); sc[j] = MFMA(kb, qf1, z); }
            } else if constexpr (k <= 10) {
                constexpr int j = k - 6;
                if (j < 4 || cg == 0) {
                    if (tile - (cg ? 3 : 8) + j < 0) {
                        asm volatile("");
#pragma unroll
                        for (int r = 0; r < 16; ++r) sc[j][r] = -1.0e30f;
                    }
#pragma unroll
                    for (int r = 0; r < 16; r += 2) m = max3(m, sc[j][r], sc[j][r + 1]);
                }
                if constexpr (j == 4) m = __builtin_fmaxf(m, xhalf(m));
            } else if constexpr (k <= 20) {
                constexpr int e = k - 11, j = e >> 1, h = e & 1;
                const bf16x8 va = fa;
                if constexpr (e < 9) fa = kv((e + 1) >> 1, 2 + ((e + 1) & 1));
                if (j < 4 || cg == 0) {
#pragma unroll
                    for (int r = 0; r < 8; ++r) sc[j][8 * h + r] = __builtin_amdgcn_exp2f(sc[j][8 * h + r] - m);
                    o = MFMA(va, __builtin_bit_cast(bf16x8, pack8(sc[j], h)), e == 0 ? f32x16{} : o);
                }
            } else if constexpr (k == 22) {
                if (cg == 0) {
                    if (g & 1) o[1] = padl ? m : o[1]; else o[13] = padl ? m : o[13];
#pragma unroll
                    for (int c = 0; c < 4; ++c) scr[c * 64] = f32x4{o[4 * c], o[4 * c + 1], o[4 * c + 2], o[4 * c + 3]};
                }
            }
        };
        auto gaddr = [&](int ks) {
            const int st = ks >> 2;
            const int slot = st == 0 ? gslot(2) : st == 1 ? gslot(3) : st == 2 ? L_R : st == 3 ? gslot(0) : st == 4 ? gslot(1) : st == 5 ? gslot(2) : gslot(3);
            return ga.at(slot + (ks & 3) * 3584);
        };
        pipe<112, 5>([&](auto ic) { constexpr int i = ic.v;
                         bf16x8 a;
                         if ((i & 3) < 3 || cg == 0) { lds_ptr r = gaddr(i >> 2) + 32 * (i & 3); a = trfrag(r, r + 896); } else asm("" : "=v"(a));
                         return a;
                     },
                     [&](auto ic, bf16x8 a) { constexpr int i = ic.v;
                         if ((i & 3) < 3 || cg == 0) acc[i & 3] = MFMA(a, xf[i >> 2], acc[i & 3]);
                         if ((i & 3) == 2 * cg) gdma(i >> 4, (i >> 2) & 3);
                         if ((i & 15) == 15) ghook(i >> 4);
                     });
    if constexpr (cg == 0) WAIT_SEL(p3i, 6, "+v"(csg[0]), "+v"(csg[1]), "+v"(csg[2]), "+v"(csg[3]));
    else WAIT_SEL(p3i, 6, "+v"(csg[0]), "+v"(csg[1]), "+v"(csg[2]));
    {
        unsigned csv[4];
        bool ok = true;
#pragma unroll
        for (int t = 0; t < 4 - cg; ++t) { csv[t] = csg[t][0]; ok = ok && gran_ok(csg[t], tag); }
        if (!__all(ok)) {
#pragma unroll
            for (int t = 0; t < 4 - cg; ++t) csv[t] = gran_spin(rs, csgo + t * 512, tag);
        }
#pragma unroll
        for (int t = 0; t < 4 - cg; ++t) acc[t] = MFMA(__builtin_bit_cast(bf16x8, u32x4{csv[t], csv[t], 0u, 0u}), mfr, acc[t]);
    }
    STAMP(13);
    {
        const float gc1 = -2.0f * 0.7978845608028654f * 1.4426950408889634f * rstd, gc2 = gc1 * 0.044715f * rstd * rstd;
#pragma unroll
        for (int t = 0; t < 4; ++t) {
            if (t < 3 || cg == 0) {
                u32x4 hv;
#pragma unroll
                for (int e = 0; e < 4; ++e)
                    hv[e] = pk2(gelu_mul(acc[t][2 * e], acc[t][8 + 2 * e], gc1, gc2, ir2), gelu_mul(acc[t][2 * e + 1], acc[t][9 + 2 * e], gc1, gc2, ir2));
                stws(rs, WS_H + (tile * 112 + 7 * g + 4 * cg + t) * 1024 + lane * 16, hv);
                if (t < 1) {
#pragma unroll
                    for (int j = 0; j < 4; ++j) issue_p(3 + 2 * t + (j >> 1), j & 1);
                }
                SBAR();
            }
        }
    }
    STAMP(14);
    u64 hp;
    sfor<0, 23>([&](auto kc) {
        side(kc);
        if (kc.v == 7) {
            WAITV(0);
            set_flag(p.ws, WS_HF + ((tile * 16 + g) * 2 + cg) * 64, tag, lane);
            STAMP(15);
        }
        if (kc.v == 18) hp = poll_issue(p.ws, hfo);
        if (kc.v == 7 || kc.v == 10 || kc.v == 18) SBAR();
    });
    STAMP(16);
    POLL_CHECK(p3i, 0, hp, hfo);
    STAMP(17);
    if (p3) {
#pragma unroll
        for (int i = 0; i < 14; ++i) bf[i] = ldws(rs, hb + i * 1024);
    }
    BAR();
    STAMP(18);
    if (cg == 1) {
        f32x16 oa;
#pragma unroll
        for (int c = 0; c < 4; ++c) { f32x4 v = scr[c * 64]; oa[4 * c] = v[0]; oa[4 * c + 1] = v[1]; oa[4 * c + 2] = v[2]; oa[4 * c + 3] = v[3]; }
        float ma = (g & 1) ? oa[1] : oa[13], la = (g & 1) ? oa[0] : oa[12], lb = (g & 1) ? o[0] : o[12];
        float ma2 = xhalf(ma), la2 = xhalf(la), lb2 = xhalf(lb);
        ma = padl ? ma : ma2; la = padl ? la : la2; lb = padl ? lb : lb2;
        const float mm = __builtin_fmaxf(ma, m);
        float wa = __builtin_amdgcn_exp2f(ma - mm), wb = __builtin_amdgcn_exp2f(m - mm);
        const float inv = __builtin_amdgcn_rcpf(la * wa + lb * wb);
        wa *= inv; wb *= inv;
#pragma unroll
        for (int r = 0; r < 16; ++r) {
            o[r] = oa[r] * wa + o[r] * wb;
            if (r < 4) o[r] *= keeplo;
            if (r >= 12) o[r] *= keephi;
        }
        const unsigned fo = WS_O + (tile * 16 + g) * 2048 + lane * 16;
        stws(rs, fo, pack8(o, 0)); stws(rs, fo + 1024, pack8(o, 1));
        if (!p3) { WAITV(0); set_flag(p.ws, WS_OF + (tile * 16 + g) * 64, tag, lane); }
    }
    }
    };
    if (cg == 0) phase1(IC<0>{}); else phase1(IC<1>{});
    STAMP(19);
    if (!p3) {
        if (blockIdx.x == 255 && w == 0) {
#pragma unroll
            for (int j = 0; j < 4; ++j) { int id = j * 64 + lane; wait_flags(p.ws, WS_KVF + ((id >> 4) * 64 + (id & 15)) * 64, tag); }
            set_flag(p.ws, WS_EPOCH, tag, lane);
        }
        STAMP_OUT;
        return;
    }
    {
        const unsigned ofo = WS_OF + (tt * 16 + 4 * kq + (lane & 3)) * 64;
        const unsigned ob = WS_O + (tt * 16 + 4 * kq) * 2048 + lane * 16;
        const u32x4 xres = *(const u32x4*)(p.x + (size_t)(rb * 64 + (tid >> 3)) * 896 + cb * 128 + (tid & 7) * 16);
        u64 op;
        f32x16 af[2], as[2];
        af[0] = f32x16{}; af[1] = f32x16{}; as[0] = f32x16{}; as[1] = f32x16{};
        const int lo = kq * 4096 + (4 * hi + q4) * 128 + 32 * g16 + 8 * p4;
        const int n0 = ((q4 >> 1) & 1) * 64;
        const LB pa(smb + lo + n0), pb(smb + lo + (64 - n0));
        auto phook = [&](int st) {
            if (st == 0) STAMP(21); if (st == 6) STAMP(22); if (st == 8) STAMP(23); if (st == 13) STAMP(24); if (st == 17) STAMP(25);
            if (st == 2 && cg) { WAITV(12); set_flag(p.ws, WS_OF + (tile * 16 + g) * 64, tag, lane); }
            if (st == 3 || st == 4 || st == 6 || st == 10 || st == 11) WAITV(12);
            else if (st == 5) { WAITV(12); op = poll_issue(p.ws, ofo); }
            else if (st == 7) POLL_CHECK(p3i, 6, op, ofo);
            else if (st == 9 || st == 12) WAITV(10); else if (st == 13) WAITV(6); else if (st == 14) WAITV(0);
            if (st < 15) RAWBAR();
        };
        pipe<72, 5>([&](auto ic) { constexpr int i = ic.v; lds_ptr r = ((i & 1) ? pb : pa).at(pslot(i >> 2) + ((i >> 1) & 1) * 2048); return trfrag(r, r + 1024); },
                    [&](auto ic, bf16x8 a) { constexpr int i = ic.v;
                        constexpr int ks = i >> 1, st = i >> 2, j = i & 3;
                        const bf16x8 b = __builtin_bit_cast(bf16x8, bf[ks % 28]);
                        if (ks < 28) af[i & 1] = MFMA(a, b, af[i & 1]); else as[i & 1] = MFMA(a, b, as[i & 1]);
                        if (st <= 12 && j < 2) issue_p(st + 5, j);
                        if constexpr (st < 7 && (j & 1)) bf[14 + ks] = ldws(rs, hb + (14 + ks) * 1024);
                        if constexpr (st >= 8 && st <= 11 && (j & 1)) bf[ks - 16] = ldws(rs, ob + (ks - 16) * 1024);
                        if (j == 3) phook(st);
                    });
        BAR();
        {
            lds_ptr d = sm + kq * 17408 + (mh * 32 + (lane & 31)) * 272 + hi * 16;
#pragma unroll
            for (int nh = 0; nh < 2; ++nh)
#pragma unroll
                for (int c = 0; c < 4; ++c) {
                    *(LAS f32x4*)(d + nh * 128 + c * 32) = f32x4{af[nh][4 * c], af[nh][4 * c + 1], af[nh][4 * c + 2], af[nh][4 * c + 3]};
                    *(LAS f32x4*)(d + 69632 + nh * 128 + c * 32) = f32x4{as[nh][4 * c], as[nh][4 * c + 1], as[nh][4 * c + 2], as[nh][4 * c + 3]};
                }
        }
        BAR();
        {
            lds_ptr d = sm + (tid >> 3) * 272 + (tid & 7) * 32;
            f32x4 f0 = {}, f1 = {}, s0 = {}, s1 = {};
#pragma unroll
            for (int k = 0; k < 4; ++k) {
                f0 += *(LAS f32x4*)(d + k * 17408); f1 += *(LAS f32x4*)(d + k * 17408 + 16);
                s0 += *(LAS f32x4*)(d + 69632 + k * 17408); s1 += *(LAS f32x4*)(d + 69632 + k * 17408 + 16);
            }
            float r[8];
#pragma unroll
            for (int e = 0; e < 8; ++e) {
                const float f = e < 4 ? f0[e & 3] : f1[e & 3], s = e < 4 ? s0[e & 3] : s1[e & 3];
                const float gt = f * __builtin_amdgcn_rcpf(1.0f + __builtin_amdgcn_exp2f(-1.4426950408889634f * f));
                const float xv = (e & 1) ? bfhi(xres[e >> 1]) : bflo(xres[e >> 1]);
                r[e] = __builtin_fmaf(s, gt, xv);
            }
            u32x4 ov = {pk2(r[0], r[1]), pk2(r[2], r[3]), pk2(r[4], r[5]), pk2(r[6], r[7])};
            *(u32x4*)(p.out + (size_t)(rb * 64 + (tid >> 3)) * 896 + cb * 128 + (tid & 7) * 16) = ov;
            STAMP(26); STAMP_OUT;
        }
    }
}

extern "C" void kernel_launch(void* const* d_in, const int* in_sizes, int n_in, void* d_out, int out_size, void* d_ws, size_t ws_size,
                              hipStream_t stream) {
    (void)in_sizes; (void)n_in; (void)out_size; (void)ws_size;
    Params p;
    p.x = (const char*)d_in[0]; p.wqkv = (const char*)d_in[1]; p.wo = (const char*)d_in[2];
    p.wg = (const char*)d_in[4]; p.wu = (const char*)d_in[5]; p.wd = (const char*)d_in[6];
    p.out = (char*)d_out; p.ws = (char*)d_ws;
    (void)hipFuncSetAttribute((const void*)fused_kernel, hipFuncAttributeMaxDynamicSharedMemorySize, LDS_BYTES);
    fused_kernel<<<256, 512, LDS_BYTES, stream>>>(p);
}
```

```cpp
#include <hip/hip_runtime.h>
#include <hip/hip_bf16.h>

typedef __attribute__((ext_vector_type(8))) __bf16 bf16x8;
typedef __attribute__((ext_vector_type(2))) __bf16 bf16x2;
typedef __attribute__((ext_vector_type(16))) float f32x16;
typedef __attribute__((ext_vector_type(4))) float f32x4;
typedef __attribute__((ext_vector_type(2))) float f32x2;
typedef __attribute__((ext_vector_type(4))) unsigned u32x4;
typedef __attribute__((ext_vector_type(2))) unsigned u32x2;
typedef short v4i16_t __attribute__((ext_vector_type(4)));
typedef short v8i16_t __attribute__((ext_vector_type(8)));
typedef unsigned long long u64;
#define LAS __attribute__((address_space(3)))
typedef LAS char* lds_ptr;
typedef __attribute__((address_space(1))) u64 gu64;
#define DI __device__ __forceinline__
#define MFMA(a, b, c) __builtin_amdgcn_mfma_f32_32x32x16_bf16(a, b, c, 0, 0, 0)
#define RLX_AGENT __ATOMIC_RELAXED, __HIP_MEMORY_SCOPE_AGENT
#define WAITV(N) asm volatile("s_waitcnt vmcnt(" #N ")" ::: "memory")
#define BAR() asm volatile("s_waitcnt lgkmcnt(0)\n\ts_barrier" ::: "memory")
template <int N> DI void waitv() { asm volatile("s_waitcnt vmcnt(%0)" :: "i"(N) : "memory"); }

#define STAMPS 0

constexpr int AUX_SC1 = 16;
constexpr unsigned WS_EPOCH = 0;
constexpr unsigned WS_KVF = 4096;
constexpr unsigned WS_OF = WS_KVF + 65536;
constexpr unsigned WS_HF = WS_OF + 65536;
constexpr unsigned WS_CS = WS_HF + 131072;
constexpr unsigned WS_STAMP = 512 * 1024;
constexpr unsigned WS_KV = 1u << 20,
                    WS_O = 7u << 20, WS_H = 9u << 20, WS_END = 16u << 20;
constexpr int L_R = 0, L_S0 = 48640, L_GS = 28800, L_UP = 14336 + 128;
constexpr int L_Q = 32768, L_V = 0, L_K = 49152;
constexpr int L_CS_BIG = 43008, L_ST = 46592, L_RS = 41984;
constexpr int LDS_BYTES = 163840;
constexpr int CODE_BYTES = 49152;
DI int kvaddr(int fr) { return ((fr & 2) ? L_V : L_K) + (fr >> 2) * 2048 + (fr & 1) * 1024; }
DI constexpr int gslot(int i) { return L_S0 + i * L_GS; }
DI constexpr int pslot(int s) { return (s % 10 < 5 ? s % 10 + 5 : s % 10 < 7 ? s % 10 - 2 : s % 10 - 7) * 16384; }

struct Params {
    const char* x; const char* wqkv; const char* wo; const char* wg; const char* wu; const char* wd;
    char* out; char* ws;
};

DI void glds(const char* sbase, unsigned voff, unsigned lds) {
    unsigned keep;
    asm volatile("s_mov_b32 %0, m0\n\ts_mov_b32 m0, %3\n\ts_nop 0\n\tglobal_load_lds_dwordx4 %1, %2\n\ts_mov_b32 m0, %0"
                 : "=&s"(keep) : "v"(voff), "s"(sbase), "s"(lds) : "memory");
}
DI void touch(const char* sbase, unsigned voff, unsigned lds) {
    unsigned keep;
    asm volatile("s_mov_b32 %0, m0\n\ts_mov_b32 m0, %3\n\ts_nop 0\n\tglobal_load_lds_dword %1, %2 sc1\n\ts_mov_b32 m0, %0"
                 : "=&s"(keep) : "v"(voff), "s"(sbase), "s"(lds) : "memory");
}
DI v4i16_t trr(lds_ptr p) { return __builtin_amdgcn_ds_read_tr16_b64_v4i16((LAS v4i16_t*)p); }
DI bf16x8 trfrag(lds_ptr p0, lds_ptr p1) {
    v4i16_t a = trr(p0), b = trr(p1);
    return __builtin_bit_cast(bf16x8, __builtin_shufflevector(a, b, 0, 1, 2, 3, 4, 5, 6, 7));
}
DI unsigned pk2(float a, float b) { f32x2 v = {a, b}; return __builtin_bit_cast(unsigned, __builtin_convertvector(v, bf16x2)); }
DI u32x4 pack8(const f32x16& c, int s) {
    u32x4 r = {pk2(c[8 * s], c[8 * s + 1]), pk2(c[8 * s + 2], c[8 * s + 3]), pk2(c[8 * s + 4], c[8 * s + 5]), pk2(c[8 * s + 6], c[8 * s + 7])};
    return r;
}
DI float bflo(unsigned u) { return __uint_as_float(u << 16); }
DI float bfhi(unsigned u) { return __uint_as_float(u & 0xffff0000u); }
DI float rbf(float a) { return bflo(pk2(a, a) & 0xffffu); }
DI float max3(float a, float b, float c) {
#pragma clang fp reassociate(on)
    return __builtin_amdgcn_fmed3f(__builtin_amdgcn_fmed3f(a, b, __builtin_inff()), c, __builtin_inff());
}
DI float xhalf(float v) {
    auto r = __builtin_amdgcn_permlane32_swap(__float_as_uint(v), __float_as_uint(v), false, false);
    return __uint_as_float((threadIdx.x & 32) ? r[0] : r[1]);
}
DI u32x4 ldws(__amdgpu_buffer_rsrc_t rs, unsigned off) { return __builtin_amdgcn_raw_buffer_load_b128(rs, (int)off, 0, AUX_SC1); }
DI void stws(__amdgpu_buffer_rsrc_t rs, unsigned off, u32x4 v) { __builtin_amdgcn_raw_buffer_store_b128(v, rs, (int)off, 0, AUX_SC1); }
DI void set_flag(char* ws, unsigned off, u64 tag, int lane) {
    (void)lane;
    __hip_atomic_store((gu64*)(ws + off), tag, RLX_AGENT);
}
DI void wait_flags(char* ws, unsigned off, u64 tag) {
    for (int spin = 0; spin < (1 << 20); ++spin) {
        u64 v = __hip_atomic_load((gu64*)(ws + off), RLX_AGENT);
        if (__all(v == tag)) break;
        __builtin_amdgcn_s_sleep(2);
    }
    __builtin_amdgcn_fence(__ATOMIC_ACQUIRE, "wavefront");
}
DI u64 poll_issue(char* ws, unsigned off) {
    u64 v;
    asm volatile("global_load_dwordx2 %0, %1, %2 sc1" : "=v"(v) : "v"(off), "s"(ws) : "memory");
    return v;
}
#define WAIT_SEL(c, NA, ...) asm volatile("s_cmp_lg_u32 %[sel], 0\n\ts_cbranch_scc1 1f\n\ts_waitcnt vmcnt(0)\n1:\n\ts_waitcnt vmcnt(" #NA ")" \
                                          : __VA_ARGS__ : [sel] "s"(c) : "scc", "memory")
#define POLL_CHECK(c, N, v, off) do { WAIT_SEL(c, N, "+v"(v)); if (!__all(v == tag)) wait_flags(p.ws, off, tag); } while (0)
DI u32x4 gran_issue(char* ws, unsigned off) {
    u32x4 v;
    asm volatile("global_load_dwordx4 %0, %1, %2 sc1" : "=v"(v) : "v"(off), "s"(ws) : "memory");
    return v;
}
DI bool gran_ok(u32x4 v, u64 tag) { return v[1] == (unsigned)tag && v[3] == (unsigned)(tag >> 32); }
DI unsigned gran_spin(__amdgpu_buffer_rsrc_t rs, unsigned off, u64 tag) {
    u32x4 v = ldws(rs, off);
    for (int spin = 0; spin < (1 << 20) && !__all(gran_ok(v, tag)); ++spin) { __builtin_amdgcn_s_sleep(2); v = ldws(rs, off); }
    return v[0];
}
#define STAMP(i)
#define STAMP_DECL
#define STAMP_OUT

#define SBAR() __builtin_amdgcn_sched_barrier(0)
#define RAWBAR() asm volatile("s_barrier" ::: "memory")
template <int V> struct IC { static constexpr int v = V; constexpr operator int() const { return V; } };
template <int I, int N, class F>
DI void sfor(F&& f) { if constexpr (I < N) { f(IC<I>{}); sfor<I + 1, N>(f); } }
template <int N, int D, class LD, class MM>
DI void pipe(LD ld, MM mm) {
    bf16x8 buf[D];
    sfor<0, D - 1>([&](auto i) { buf[i.v] = ld(i); });
    SBAR();
    sfor<0, N>([&](auto i) {
        if constexpr (i.v + D - 1 < N) buf[(i.v + D - 1) % D] = ld(IC<i.v + D - 1>{});
        SBAR();
        mm(i, buf[i.v % D]);
        SBAR();
    });
}
struct LB {
    unsigned b[3];
    DI LB(unsigned a) { b[0] = a; b[1] = a + 0xc000; b[2] = a + 0x18000 + 0x2000; asm volatile("" : "+v"(b[0]), "+v"(b[1]), "+v"(b[2])); }
    DI lds_ptr at(int off) const { return off < 0xc000 ? (lds_ptr)(size_t)b[0] + off : off < 0x1a000 ? (lds_ptr)(size_t)b[1] + (off - 0xc000) : (lds_ptr)(size_t)b[2] + (off - 0x1a000); }
};
DI float gelu_mul(float a, float b, float c1, float c2, float ir2) {
    float e = __builtin_amdgcn_exp2f(a * __builtin_fmaf(a * a, c2, c1));
    return a * b * __builtin_amdgcn_rcpf(__builtin_fmaf(e, ir2, ir2));
}

__global__ __launch_bounds__(512, 2) void fused_kernel(Params p) {
    extern __shared__ __attribute__((aligned(16))) char smem_[];
    lds_ptr sm = (lds_ptr)smem_;
    const unsigned smb = (unsigned)(size_t)sm;
    const int tid = threadIdx.x, lane = tid & 63;
    const int w = __builtin_amdgcn_readfirstlane(tid >> 6);
    const int hi = lane >> 5;
    const int xcd = blockIdx.x & 7, li = blockIdx.x >> 3;
    const int rt = 4 * (xcd >> 1) + (li >> 3), g = 8 * (xcd & 1) + (li & 7);
    const bool p3 = li < 28;
    const int rb = 4 * xcd + li / 7, cb = li % 7;
    const int rg = w & 3, cg = w >> 2;
    const int p3i = 28 - li > 0 ? 28 - li : 0;
    const __amdgpu_buffer_rsrc_t rs = __builtin_amdgcn_make_buffer_rsrc((void*)p.ws, 0, (int)WS_END, 0x00020000);
    STAMP_DECL; STAMP(0);

    const char* wq = p.wqkv + (56 * g - 8 * (g & 1));
    const char* wgb = p.wg + 224 * g;
    const char* wub = p.wu + 224 * g;
    u64 tag_ = poll_issue(p.ws, WS_EPOCH);
    {
        const char* xb = p.x + (size_t)(rt * 128 + w * 16) * 896;
        const unsigned xo = (lane >> 3) * 896 + ((lane & 7) ^ (lane >> 4)) * 16;
#pragma unroll
        for (int j = 0; j < 14; ++j)
            glds(xb + (j / 7) * 8 * 896 + (j % 7) * 128, j < 7 ? xo : xo ^ 64, smb + L_S0 + w * 14336 + j * 1024);
    }
    const unsigned qo = (lane >> 2) * 2688 + (lane & 3) * 16;
    auto issue_q = [&](int half, int pc) {
        if (w < 7) glds(wq + (pc >> 1) * 896 + (size_t)(half * 224 + (w * 2 + (pc & 1)) * 16) * 2688, qo,
                        smb + (half ? L_S0 : L_R) + (pc >> 1) * 14336 + (w * 2 + (pc & 1)) * 1024);
    };
#pragma unroll
    for (int j = 0; j < 6; ++j) issue_q(0, j);
    u32x4 cw[4];
    if (rt < 3 && w < 7) {
#pragma unroll
        for (int j = 0; j < 4; ++j)
            asm volatile("global_load_dwordx4 %0, %1, %2" : "=v"(cw[j]) : "v"(qo), "s"(wq + rt * 896 + (size_t)(w * 64 + j * 16) * 2688) : "memory");
    }
    if (w == 7) {
        const unsigned d = smb + L_ST + 7 * 256;
        touch(p.ws, lane * 262144, d);
        const unsigned o64 = lane < 24 ? lane * 65536 : 0;
        touch(p.wg, o64, d); touch(p.wu, o64, d); touch(p.wd, o64, d); touch(p.out, o64, d);
        touch(p.wo, lane < 6 ? lane * 65536 : 0, d);
    }
    const int codew = li == 31 && w < CODE_BYTES / 8192;
    if (codew) {
        const char* pc;
        asm volatile("s_getpc_b64 %0" : "=s"(pc));
        touch(pc + w * 8192, lane * 128, smb + L_CS_BIG + w * 512);
    }
    unsigned goff[4];
    const int gp0 = cg ? 12 + rg * 4 : rg * 3;
#pragma unroll
    for (int j = 0; j < 4; ++j) {
        const int pc = gp0 + j, i = (pc % 14) * 64 + lane, row = i / 14, c = i - row * 14;
        goff[j] = row * 3584 + c * 16;
    }
    auto issue_g = [&](int st, int slot, int n) {
        const int pc = gp0 + n;
        if (n < 3 || cg) glds((pc >= 14 ? wub : wgb) + (size_t)st * 64 * 3584, goff[n], smb + slot + (pc >= 14 ? L_UP - 14336 : 0) + pc * 1024);
    };
    const unsigned poff = ((w & 1) * 16 + (lane >> 3)) * 896 + cb * 128 + (((lane & 7) ^ ((lane >> 2) & 4)) * 16);
    auto issue_p = [&](int st, int pc) {
        if (p3) {
            const int kq = w >> 1;
            const int hh = 4 * kq + st - 14;
            const char* b = st < 14 ? p.wd + (size_t)(448 * kq + 32 * st) * 896 : p.wo + (size_t)(28 * hh - 4 * (hh & 1)) * 896;
            glds(b + pc * 8 * 896, poff, smb + pslot(st) + w * 2048 + pc * 1024);
        }
    };
    u64 tag;
    auto cs_partial = [&]() {
        float cs[8];
#pragma unroll
        for (int e = 0; e < 4; ++e) {
            cs[2 * e] = (bflo(cw[0][e]) + bflo(cw[1][e])) + (bflo(cw[2][e]) + bflo(cw[3][e]));
            cs[2 * e + 1] = (bfhi(cw[0][e]) + bfhi(cw[1][e])) + (bfhi(cw[2][e]) + bfhi(cw[3][e]));
        }
#pragma unroll
        for (int e = 0; e < 8; ++e) {
            cs[e] += __builtin_bit_cast(float, __builtin_amdgcn_update_dpp(0, __builtin_bit_cast(int, cs[e]), 0x124, 0xf, 0xf, false));
            cs[e] += __builtin_bit_cast(float, __builtin_amdgcn_update_dpp(0, __builtin_bit_cast(int, cs[e]), 0x128, 0xf, 0xf, false));
        }
        {
            LAS f32x4* d = (LAS f32x4*)(sm + L_CS_BIG + w * 512 + (lane >> 4) * 128 + (lane & 3) * 32);
            d[0] = f32x4{cs[0], cs[1], cs[2], cs[3]}; d[1] = f32x4{cs[4], cs[5], cs[6], cs[7]};
        }
    };
    auto cs_publish = [&]() {
        float v = 0.f;
        LAS float* d = (LAS float*)(sm + L_CS_BIG + (lane & 31) * 4 + hi * 1792);
#pragma unroll
        for (int j = 0; j < 14; ++j) v += d[j * 32];
        v += xhalf(v);
        const unsigned h_ = pk2(v, v) & 0xffffu;
        const unsigned pv = h_ | (pk2(v - bflo(h_), 0.f) << 16);
        stws(rs, WS_CS + ((g * 10 + rt) * 32 + (lane & 31)) * 16, u32x4{pv, (unsigned)tag, pv, (unsigned)(tag >> 32)});
    };
    STAMP(1);
    {
        const int sel = (rt < 3 && w < 7) ? 10 : 6;
        asm volatile("s_cmp_eq_u32 %1, 10\n\ts_cbranch_scc1 1f\n\ts_cmp_eq_u32 %1, 6\n\ts_cbranch_scc1 2f\n\ts_waitcnt vmcnt(0)\n2:\n\ts_waitcnt vmcnt(6)\n1:\n\ts_waitcnt vmcnt(10)"
                     : "+v"(tag_) : "s"(sel) : "scc", "memory");
    }
    BAR();
    STAMP(2);
    tag = tag_ + 1;
    const int tile = rt * 4 + rg;
    const int q4 = (lane >> 2) & 3, p4 = lane & 3, g16 = (lane >> 4) & 1;
    const int kq = w >> 1, mh = w & 1, tt = p3 ? rb * 2 + mh : 0;
    const unsigned hfo = WS_HF + ((tt * 16 + 4 * kq) * 2 + (lane & 7)) * 64;
    const unsigned hb = WS_H + (tt * 112 + 28 * kq) * 1024 + lane * 16;
    u32x4 bf[28];
    auto phase1 = [&](auto cgc) {
    constexpr int cg = cgc.v;
    constexpr int NG = 3 + cg;
    auto issue_gc = [&](int st, int slot, int n) { if (n < NG) issue_g(st, slot, n); };
    bf16x8 xf[28];
    {
        const int t = lane & 31;
        lds_ptr xr = sm + L_S0 + (rg * 4 + (t >> 3)) * 7168 + (t & 7) * 128;
        const int sw = (t >> 1) & 7;
#pragma unroll
        for (int s_ = 0; s_ < 28; ++s_) { const int s = (s_ + 14 * cg) % 28, c = 2 * s + hi; xf[s] = *(LAS bf16x8*)(xr + (c >> 3) * 1024 + (((c ^ sw) & 7) * 16)); }
    }
    const bool csd = rt >= 3 && rt < 10, csw = csd && w < 7;
    {
        float s1a[4] = {0.f, 0.f, 0.f, 0.f}, s2a[4] = {0.f, 0.f, 0.f, 0.f};
        const bf16x2 ones = {(__bf16)1.0f, (__bf16)1.0f};
        auto stat = [&](const bf16x8& xv) {
            u32x4 u = __builtin_bit_cast(u32x4, xv);
#pragma unroll
            for (int e = 0; e < 4; ++e) {
                const unsigned ue = u[e];
                const bf16x2 v = __builtin_bit_cast(bf16x2, ue);
                s1a[e] = __builtin_amdgcn_fdot2_f32_bf16(v, ones, s1a[e], false);
                s2a[e] = __builtin_amdgcn_fdot2_f32_bf16(v, v, s2a[e], false);
            }
        };
        if (cg == 0) {
#pragma unroll
            for (int s = 0; s < 14; ++s) stat(xf[s]);
        } else {
#pragma unroll
            for (int s = 14; s < 28; ++s) stat(xf[s]);
        }
        f32x2 sv = {(s1a[0] + s1a[1]) + (s1a[2] + s1a[3]), (s2a[0] + s2a[1]) + (s2a[2] + s2a[3])};
        sv[0] += xhalf(sv[0]); sv[1] += xhalf(sv[1]);
        STAMP(3);
        if (codew) WAITV(1); else WAITV(0);
        *(LAS f32x2*)(sm + L_ST + w * 256 + (lane & 31) * 8) = sv;
    }
    if (rt < 3 && w < 7) { asm volatile("" : "+v"(cw[0]), "+v"(cw[1]), "+v"(cw[2]), "+v"(cw[3])); cs_partial(); }
    BAR();
    if (rt < 3 && w == 7) cs_publish();
    f32x16 acc[4];
    const float keeplo = ((g & 1) && hi == 0) ? 0.f : 1.f, keephi = (!(g & 1) && hi == 1) ? 0.f : 1.f;
    float rstd, ir2;
    bf16x8 mfr;
#pragma unroll
    for (int t = 0; t < 4; ++t) acc[t] = f32x16{};
    {
        STAMP(4);
        const unsigned cso = WS_CS + (g * 10 * 32 + (lane & 31)) * 16;
        u32x4 cg0v, cg1v;
        const LB qa(smb + (8 * hi + q4) * 64 + 32 * g16 + 8 * p4 + cg * 14336);
        auto qops = [&](int half, int n) {
            if (half == 0 && n < 6) issue_q(1, n);
            if (half == 0 && n >= 10) issue_gc(0, gslot(2), n - 10);
            if (half == 1 && n < 4) issue_gc(1, gslot(3), n);
            if (half == 1 && n == 6) { cg0v = gran_issue(p.ws, cso + cg * 512); cg1v = gran_issue(p.ws, cso + 1024); }
            if (half == 1 && n >= 8 && n < 12) issue_gc(2, L_R, n - 8);
        };
        sfor<0, 2>([&](auto hc) {
            constexpr int half = hc.v, base = half ? L_S0 : L_R;
            pipe<28, 5>([&](auto ic) { constexpr int i = ic.v;
                            bf16x8 a;
                            if ((i & 1) == 0 || cg) { lds_ptr r = qa.at(base + (i >> 1) * 1024 + (i & 1) * 14336); a = trfrag(r, r + 256); } else asm("" : "=v"(a));
                            return a;
                        },
                        [&](auto ic, bf16x8 a) { constexpr int i = ic.v;
                            if (i & 1) { if (cg) acc[1] = MFMA(xf[half * 14 + (i >> 1)], a, acc[1]); }
                            else { acc[0] = MFMA(a, xf[half * 14 + (i >> 1)], acc[0]); qops(half, i >> 1); }
                        });
            if (half == 0) {
                waitv<NG>();
                BAR();
                STAMP(5);
            }
        });
        STAMP(6);
        {
            const f32x2 sa = *(LAS f32x2*)(sm + L_ST + rg * 256 + (lane & 31) * 8), sb = *(LAS f32x2*)(sm + L_ST + (rg + 4) * 256 + (lane & 31) * 8);
            const float mean = (sa[0] + sb[0]) * (1.0f / 448.0f);
            ir2 = __builtin_fmaxf((sa[1] + sb[1]) * (1.0f / 448.0f) - mean * mean, 0.f) + 1e-5f;
            rstd = __builtin_amdgcn_rsqf(ir2);
            const float mh_ = -0.5f * mean;
            const unsigned m0_ = pk2(mh_, mh_), m1_ = pk2(mh_ - bflo(m0_ & 0xffffu), mh_ - bflo(m0_ & 0xffffu));
            mfr = __builtin_bit_cast(bf16x8, u32x4{m0_, m1_, 0u, 0u});
        }
        asm volatile("s_waitcnt vmcnt(%2)" : "+v"(cg0v), "+v"(cg1v) : "i"(NG) : "memory");
        RAWBAR();
        {
            unsigned c0 = cg0v[0], c1 = cg1v[0];
            if (!__all(gran_ok(cg0v, tag) && gran_ok(cg1v, tag))) { c0 = gran_spin(rs, cso + cg * 512, tag); c1 = gran_spin(rs, cso + 1024, tag); }
            acc[0] = MFMA(__builtin_bit_cast(bf16x8, u32x4{c0, c0, 0u, 0u}), mfr, acc[0]);
            if (cg) acc[1] = MFMA(mfr, __builtin_bit_cast(bf16x8, u32x4{c1, c1, 0u, 0u}), acc[1]);
        }
        const unsigned fo = WS_KV + (tile * 16 + g) * 4096 + lane * 16;
        if (cg == 0) {
            const float qs = 0.1889822365046136f * 1.4426950408889634f * rstd;
            const float qlo = qs * keeplo, qhi = qs * keephi;
#pragma unroll
            for (int r = 0; r < 16; ++r) acc[0][r] *= r < 4 ? qlo : r >= 12 ? qhi : qs;
            LAS u32x4* qd = (LAS u32x4*)(sm + L_Q + rg * 2048 + lane * 16);
            qd[0] = pack8(acc[0], 0); qd[64] = pack8(acc[0], 1);
        } else {
            const float vpad = ((g & 1) ? ((lane & 31) < 4) : ((lane & 31) >= 28)) ? 1.f : 0.f, vkeep = 1.f - vpad;
            LAS float* rt_ = (LAS float*)(sm + L_RS + rg * 128);
            rt_[lane & 31] = rstd;
#pragma unroll
            for (int c = 0; c < 4; ++c) {
                const f32x4 rr = *(LAS f32x4*)(rt_ + 8 * c + 4 * hi);
#pragma unroll
                for (int e = 0; e < 4; ++e) { acc[1][4 * c + e] = __builtin_fmaf(acc[1][4 * c + e] * vkeep, rr[e], vpad); acc[0][4 * c + e] *= rstd; }
            }
            stws(rs, fo, pack8(acc[0], 0)); stws(rs, fo + 1024, pack8(acc[0], 1));
            stws(rs, fo + 2048, pack8(acc[1], 0)); stws(rs, fo + 3072, pack8(acc[1], 1));
        }
        acc[0] = f32x16{}; acc[1] = f32x16{};
        STAMP(7);

        STAMP(8);
    }
    u32x4 kvr[6], csg[4];
    const unsigned csgo = WS_CS + ((g * 10 + 3 + 4 * cg) * 32 + (lane & 31)) * 16;
    const char* csb = ((lane & 2) ? wub : wgb) + (rt - 3) * 32 + (lane & 1) * 16 + (size_t)(w * 64 + (lane >> 2)) * 3584;
    u64 kvp;
    unsigned kvfo;
    { int kt = 4 * rt - 8 + ((6 * w) >> 2) + (lane & 1); kt = kt < 0 ? 0 : kt; kvfo = WS_KVF + (kt * 16 + g) * 64; }
    f32x16 o;
    float m;
    LAS f32x4* scr = (LAS f32x4*)(sm + (rg < 2 ? 24576 : 65536) + rg * 4096 + lane * 16);
    const bool padl = (g & 1) ? (hi == 0) : (hi == 1);
    {
        const LB ga(smb + (8 * hi + q4) * 224 + L_UP * g16 + 8 * p4 + cg * 128);
        auto ghook = [&](int st) {
            if (st == 0) STAMP(9); if (st == 1) STAMP(10); if (st == 4) STAMP(11); if (st == 6) STAMP(12);
            if (st == 0) {
                if (csw) waitv<NG + 2>(); else waitv<NG>();
                if (cg) set_flag(p.ws, WS_KVF + (tile * 16 + g) * 64, tag, lane);
            } else if (st == 1) { if (csw) waitv<NG + 2>(); else waitv<NG>(); }
            else if (st == 2) {
                waitv<NG>();
                if (csw) { asm volatile("" : "+v"(cw[0]), "+v"(cw[1]), "+v"(cw[2]), "+v"(cw[3])); cs_partial(); }
                kvp = poll_issue(p.ws, kvfo);
                BAR();
                if (csd && w == 7) cs_publish();
                return;
            } else if (st == 3) { asm volatile("s_waitcnt vmcnt(%1)" : "+v"(kvp) : "i"(NG) : "memory"); if (!__all(kvp == tag)) wait_flags(p.ws, kvfo, tag); }
            else if (st == 4) WAITV(6);
            else if (st == 5) asm volatile("s_waitcnt lgkmcnt(0)" ::: "memory");
            RAWBAR();
        };
        auto gdma = [&](int st, int n) {
            if (st < 4) issue_gc(3 + st, gslot(st), n);
            if (st < 2 && n < 2 && csw)
                asm volatile("global_load_dwordx4 %0, %1, off" : "=v"(cw[2 * st + n]) : "v"(csb + (size_t)(2 * st + n) * 16 * 3584) : "memory");
            if (st == 4 && n < 3) {
#pragma unroll
                for (int e = 0; e < 2; ++e) {
                    const int fr = 6 * w + 2 * n + e;
                    int kt = 4 * rt - 8 + (fr >> 2); kt = kt < 0 ? 0 : kt;
                    kvr[2 * n + e] = ldws(rs, WS_KV + (kt * 16 + g) * 4096 + (fr & 3) * 1024 + lane * 16);
                }
            }
            if (st == 5 && n == 1) {
#pragma unroll
                for (int e = 0; e < 6; ++e) *(LAS u32x4*)(sm + kvaddr(6 * w + e) + lane * 16) = kvr[e];
            }
            if (st == 5 && n == 3) {
#pragma unroll
                for (int t = 0; t < 4 - cg; ++t) csg[t] = gran_issue(p.ws, csgo + t * 512);
            }
        };
        f32x16 sc[5];
        bf16x8 qf0, qf1, fa, fb;
        auto side = [&](auto kc) {
            constexpr int k = kc.v;
            auto kv = [&](int j, int part) { return *(LAS bf16x8*)(sm + kvaddr(4 * (rg + 5 * cg + (j < 4 ? j : 4 - cg)) + part) + lane * 16); };
            if constexpr (k == 0) {
                LAS bf16x8* ql_ = (LAS bf16x8*)(sm + L_Q + rg * 2048 + lane * 16);
                qf0 = ql_[0]; qf1 = ql_[64]; fa = kv(0, 0); fb = kv(0, 1); m = -3.0e38f;
            } else if constexpr (k <= 5) {
                constexpr int j = k - 1;
                const bf16x8 ka = fa, kb = fb;
                if constexpr (j < 4) { fa = kv(j + 1, 0); fb = kv(j + 1, 1); } else fa = kv(0, 2);
                f32x16 z = f32x16{};
                if constexpr (j == 0 || j == 3) {
                    const int ql = lane & 31;
                    const float big = ((j == 0) == (cg == 0)) ? 0x1p100f : 0.f;
                    const float b0 = (float)(j ? ql - 4 * hi : 4 * hi - ql - 1) * big;
#pragma unroll
                    for (int r = 0; r < 16; ++r) z[r] = __builtin_fminf(__builtin_fmaf(j ? -big : big, (float)((r & 3) + 8 * (r >> 2)), b0), 0.f);
                }
                if (j < 4 || cg == 0) { z = MFMA(ka, qf0, z); sc[j] = MFMA(kb, qf1, z); }
            } else if constexpr (k <= 10) {
                constexpr int j = k - 6;
                if (j < 4 || cg == 0) {
                    if (tile - (cg ? 3 : 8) + j < 0) {
                        asm volatile("");
#pragma unroll
                        for (int r = 0; r < 16; ++r) sc[j][r] = -1.0e30f;
                    }
#pragma unroll
                    for (int r = 0; r < 16; r += 2) m = max3(m, sc[j][r], sc[j][r + 1]);
                }
                if constexpr (j == 4) m = __builtin_fmaxf(m, xhalf(m));
            } else if constexpr (k <= 20) {
                constexpr int e = k - 11, j = e >> 1, h = e & 1;
                const bf16x8 va = fa;
                if constexpr (e < 9) fa = kv((e + 1) >> 1, 2 + ((e + 1) & 1));
                if (j < 4 || cg == 0) {
#pragma unroll
                    for (int r = 0; r < 8; ++r) sc[j][8 * h + r] = __builtin_amdgcn_exp2f(sc[j][8 * h + r] - m);
                    o = MFMA(va, __builtin_bit_cast(bf16x8, pack8(sc[j], h)), e == 0 ? f32x16{} : o);
                }
            } else if constexpr (k == 22) {
                if (cg == 0) {
                    if (g & 1) o[1] = padl ? m : o[1]; else o[13] = padl ? m : o[13];
#pragma unroll
                    for (int c = 0; c < 4; ++c) scr[c * 64] = f32x4{o[4 * c], o[4 * c + 1], o[4 * c + 2], o[4 * c + 3]};
                }
            }
        };
        auto gaddr = [&](int ks) {
            const int st = ks >> 2;
            const int slot = st == 0 ? gslot(2) : st == 1 ? gslot(3) : st == 2 ? L_R : st == 3 ? gslot(0) : st == 4 ? gslot(1) : st == 5 ? gslot(2) : gslot(3);
            return ga.at(slot + (ks & 3) * 3584);
        };
        pipe<112, 5>([&](auto ic) { constexpr int i = ic.v;
                         bf16x8 a;
                         if ((i & 3) < 3 || cg == 0) { lds_ptr r = gaddr(i >> 2) + 32 * (i & 3); a = trfrag(r, r + 896); } else asm("" : "=v"(a));
                         return a;
                     },
                     [&](auto ic, bf16x8 a) { constexpr int i = ic.v;
                         if ((i & 3) < 3 || cg == 0) acc[i & 3] = MFMA(a, xf[i >> 2], acc[i & 3]);
                         if ((i & 3) == 2 * cg) gdma(i >> 4, (i >> 2) & 3);
                         if ((i & 15) == 15) ghook(i >> 4);
                     });
    __builtin_amdgcn_s_setprio(1);
    if constexpr (cg == 0) asm volatile("s_waitcnt vmcnt(0)" : "+v"(csg[0]), "+v"(csg[1]), "+v"(csg[2]), "+v"(csg[3]) :: "memory");
    else asm volatile("s_waitcnt vmcnt(0)" : "+v"(csg[0]), "+v"(csg[1]), "+v"(csg[2]) :: "memory");
    {
        unsigned csv[4];
        bool ok = true;
#pragma unroll
        for (int t = 0; t < 4 - cg; ++t) { csv[t] = csg[t][0]; ok = ok && gran_ok(csg[t], tag); }
        if (!__all(ok)) {
#pragma unroll
            for (int t = 0; t < 4 - cg; ++t) csv[t] = gran_spin(rs, csgo + t * 512, tag);
        }
#pragma unroll
        for (int t = 0; t < 4 - cg; ++t) acc[t] = MFMA(__builtin_bit_cast(bf16x8, u32x4{csv[t], csv[t], 0u, 0u}), mfr, acc[t]);
    }
    STAMP(13);
    {
        const float gc1 = -2.0f * 0.7978845608028654f * 1.4426950408889634f * rstd, gc2 = gc1 * 0.044715f * rstd * rstd;
#pragma unroll
        for (int t = 0; t < 4; ++t) {
            if (t < 3 || cg == 0) {
                u32x4 hv;
#pragma unroll
                for (int e = 0; e < 4; ++e)
                    hv[e] = pk2(gelu_mul(acc[t][2 * e], acc[t][8 + 2 * e], gc1, gc2, ir2), gelu_mul(acc[t][2 * e + 1], acc[t][9 + 2 * e], gc1, gc2, ir2));
                stws(rs, WS_H + (tile * 112 + 7 * g + 4 * cg + t) * 1024 + lane * 16, hv);
                SBAR();
            }
        }
    }
    __builtin_amdgcn_s_setprio(0);
    STAMP(14);
    u64 hp;
    sfor<0, 23>([&](auto kc) {
        side(kc);
        if (kc.v == 6) {
            WAITV(0);
            set_flag(p.ws, WS_HF + ((tile * 16 + g) * 2 + cg) * 64, tag, lane);
            STAMP(15);
        }
        if (kc.v >= 7 && kc.v < 17) issue_p((kc.v - 7) >> 1, (kc.v - 7) & 1);
        if (kc.v == 17) {
            const int wid = li * 8 + w;
            if (wid < 245) touch(wid < 196 ? p.wd + wid * 8192 : p.wo + (wid - 196) * 8192, lane * 128, smb + 40960);
        }
        if (kc.v == 19) hp = poll_issue(p.ws, hfo);
        if (kc.v >= 6) SBAR();
    });
    STAMP(16);
    POLL_CHECK(p3i, 0, hp, hfo);
    STAMP(17);
    if (p3) {
#pragma unroll
        for (int i = 0; i < 14; ++i) bf[i] = ldws(rs, hb + i * 1024);
    }
    BAR();
    STAMP(18);
    if (cg == 1) {
        f32x16 oa;
#pragma unroll
        for (int c = 0; c < 4; ++c) { f32x4 v = scr[c * 64]; oa[4 * c] = v[0]; oa[4 * c + 1] = v[1]; oa[4 * c + 2] = v[2]; oa[4 * c + 3] = v[3]; }
        float ma = (g & 1) ? oa[1] : oa[13], la = (g & 1) ? oa[0] : oa[12], lb = (g & 1) ? o[0] : o[12];
        float ma2 = xhalf(ma), la2 = xhalf(la), lb2 = xhalf(lb);
        ma = padl ? ma : ma2; la = padl ? la : la2; lb = padl ? lb : lb2;
        const float mm = __builtin_fmaxf(ma, m);
        float wa = __builtin_amdgcn_exp2f(ma - mm), wb = __builtin_amdgcn_exp2f(m - mm);
        const float inv = __builtin_amdgcn_rcpf(la * wa + lb * wb);
        wa *= inv; wb *= inv;
#pragma unroll
        for (int r = 0; r < 16; ++r) {
            o[r] = oa[r] * wa + o[r] * wb;
            if (r < 4) o[r] *= keeplo;
            if (r >= 12) o[r] *= keephi;
        }
        const unsigned fo = WS_O + (tile * 16 + g) * 2048 + lane * 16;
        stws(rs, fo, pack8(o, 0)); stws(rs, fo + 1024, pack8(o, 1));
        if (!p3) { WAITV(0); set_flag(p.ws, WS_OF + (tile * 16 + g) * 64, tag, lane); }
    }
    }
    };
    if (cg == 0) phase1(IC<0>{}); else phase1(IC<1>{});
    STAMP(19);
    if (!p3) {
        if (blockIdx.x == 255 && w == 0) {
#pragma unroll
            for (int j = 0; j < 4; ++j) { int id = j * 64 + lane; wait_flags(p.ws, WS_KVF + ((id >> 4) * 64 + (id & 15)) * 64, tag); }
            set_flag(p.ws, WS_EPOCH, tag, lane);
        }
        STAMP_OUT;
        return;
    }
    {
        const unsigned ofo = WS_OF + (tt * 16 + 4 * kq + (lane & 3)) * 64;
        const unsigned ob = WS_O + (tt * 16 + 4 * kq) * 2048 + lane * 16;
        const u32x4 xres = *(const u32x4*)(p.x + (size_t)(rb * 64 + (tid >> 3)) * 896 + cb * 128 + (tid & 7) * 16);
        u64 op;
        f32x16 af[2], as[2];
        af[0] = f32x16{}; af[1] = f32x16{}; as[0] = f32x16{}; as[1] = f32x16{};
        const int lo = kq * 4096 + (4 * hi + q4) * 128 + 32 * g16 + 8 * p4;
        const int n0 = ((q4 >> 1) & 1) * 64;
        const LB pa(smb + lo + n0), pb(smb + lo + (64 - n0));
        auto phook = [&](int st) {
            if (st == 0) STAMP(21); if (st == 6) STAMP(22); if (st == 8) STAMP(23); if (st == 13) STAMP(24); if (st == 17) STAMP(25);
            if (st == 2 && cg) { WAITV(12); set_flag(p.ws, WS_OF + (tile * 16 + g) * 64, tag, lane); }
            if (st == 3 || st == 4 || st == 6 || st == 10 || st == 11) WAITV(12);
            else if (st == 5) { WAITV(12); op = poll_issue(p.ws, ofo); }
            else if (st == 7) POLL_CHECK(p3i, 6, op, ofo);
            else if (st == 9 || st == 12) WAITV(10); else if (st == 13) WAITV(6); else if (st == 14) WAITV(0);
            if (st < 15) RAWBAR();
        };
        pipe<72, 5>([&](auto ic) { constexpr int i = ic.v; lds_ptr r = ((i & 1) ? pb : pa).at(pslot(i >> 2) + ((i >> 1) & 1) * 2048); return trfrag(r, r + 1024); },
                    [&](auto ic, bf16x8 a) { constexpr int i = ic.v;
                        constexpr int ks = i >> 1, st = i >> 2, j = i & 3;
                        const bf16x8 b = __builtin_bit_cast(bf16x8, bf[ks % 28]);
                        if (ks < 28) af[i & 1] = MFMA(a, b, af[i & 1]); else as[i & 1] = MFMA(a, b, as[i & 1]);
                        if (st <= 12 && j < 2) issue_p(st + 5, j);
                        if constexpr (st < 7 && (j & 1)) bf[14 + ks] = ldws(rs, hb + (14 + ks) * 1024);
                        if constexpr (st >= 8 && st <= 11 && (j & 1)) bf[ks - 16] = ldws(rs, ob + (ks - 16) * 1024);
                        if (j == 3) phook(st);
                    });
        BAR();
        {
            lds_ptr d = sm + kq * 17408 + (mh * 32 + (lane & 31)) * 272 + hi * 16;
#pragma unroll
            for (int nh = 0; nh < 2; ++nh)
#pragma unroll
                for (int c = 0; c < 4; ++c) {
                    *(LAS f32x4*)(d + nh * 128 + c * 32) = f32x4{af[nh][4 * c], af[nh][4 * c + 1], af[nh][4 * c + 2], af[nh][4 * c + 3]};
                    *(LAS f32x4*)(d + 69632 + nh * 128 + c * 32) = f32x4{as[nh][4 * c], as[nh][4 * c + 1], as[nh][4 * c + 2], as[nh][4 * c + 3]};
                }
        }
        BAR();
        {
            lds_ptr d = sm + (tid >> 3) * 272 + (tid & 7) * 32;
            f32x4 f0 = {}, f1 = {}, s0 = {}, s1 = {};
#pragma unroll
            for (int k = 0; k < 4; ++k) {
                f0 += *(LAS f32x4*)(d + k * 17408); f1 += *(LAS f32x4*)(d + k * 17408 + 16);
                s0 += *(LAS f32x4*)(d + 69632 + k * 17408); s1 += *(LAS f32x4*)(d + 69632 + k * 17408 + 16);
            }
            float r[8];
#pragma unroll
            for (int e = 0; e < 8; ++e) {
                const float f = e < 4 ? f0[e & 3] : f1[e & 3], s = e < 4 ? s0[e & 3] : s1[e & 3];
                const float gt = f * __builtin_amdgcn_rcpf(1.0f + __builtin_amdgcn_exp2f(-1.4426950408889634f * f));
                const float xv = (e & 1) ? bfhi(xres[e >> 1]) : bflo(xres[e >> 1]);
                r[e] = __builtin_fmaf(s, gt, xv);
            }
            u32x4 ov = {pk2(r[0], r[1]), pk2(r[2], r[3]), pk2(r[4], r[5]), pk2(r[6], r[7])};
            *(u32x4*)(p.out + (size_t)(rb * 64 + (tid >> 3)) * 896 + cb * 128 + (tid & 7) * 16) = ov;
            STAMP(26); STAMP_OUT;
        }
    }
}

extern "C" void kernel_launch(void* const* d_in, const int* in_sizes, int n_in, void* d_out, int out_size, void* d_ws, size_t ws_size,
                              hipStream_t stream) {
    (void)in_sizes; (void)n_in; (void)out_size; (void)ws_size;
    Params p;
    p.x = (const char*)d_in[0]; p.wqkv = (const char*)d_in[1]; p.wo = (const char*)d_in[2];
    p.wg = (const char*)d_in[4]; p.wu = (const char*)d_in[5]; p.wd = (const char*)d_in[6];
    p.out = (char*)d_out; p.ws = (char*)d_ws;
    (void)hipFuncSetAttribute((const void*)fused_kernel, hipFuncAttributeMaxDynamicSharedMemorySize, LDS_BYTES);
    fused_kernel<<<256, 512, LDS_BYTES, stream>>>(p);
}
```

```cpp
#include <hip/hip_runtime.h>
#include <hip/hip_bf16.h>

typedef __attribute__((ext_vector_type(8))) __bf16 bf16x8;
typedef __attribute__((ext_vector_type(2))) __bf16 bf16x2;
typedef __attribute__((ext_vector_type(16))) float f32x16;
typedef __attribute__((ext_vector_type(4))) float f32x4;
typedef __attribute__((ext_vector_type(2))) float f32x2;
typedef __attribute__((ext_vector_type(4))) unsigned u32x4;
typedef __attribute__((ext_vector_type(2))) unsigned u32x2;
typedef short v4i16_t __attribute__((ext_vector_type(4)));
typedef short v8i16_t __attribute__((ext_vector_type(8)));
typedef unsigned long long u64;
#define LAS __attribute__((address_space(3)))
typedef LAS char* lds_ptr;
typedef __attribute__((address_space(1))) u64 gu64;
#define DI __device__ __forceinline__
#define MFMA(a, b, c) __builtin_amdgcn_mfma_f32_32x32x16_bf16(a, b, c, 0, 0, 0)
#define RLX_AGENT __ATOMIC_RELAXED, __HIP_MEMORY_SCOPE_AGENT
#define WAITV(N) asm volatile("s_waitcnt vmcnt(" #N ")" ::: "memory")
#define BAR() asm volatile("s_waitcnt lgkmcnt(0)\n\ts_barrier" ::: "memory")
template <int N> DI void waitv() { asm volatile("s_waitcnt vmcnt(%0)" :: "i"(N) : "memory"); }

#define STAMPS 0

constexpr int AUX_SC1 = 16;
constexpr unsigned WS_EPOCH = 0;
constexpr unsigned WS_KVF = 4096;
constexpr unsigned WS_OF = WS_KVF + 65536;
constexpr unsigned WS_HF = WS_OF + 65536;
constexpr unsigned WS_CS = WS_HF + 131072;
constexpr unsigned WS_STAMP = 512 * 1024;
constexpr unsigned WS_KV = 1u << 20,
                    WS_O = 7u << 20, WS_H = 9u << 20, WS_END = 16u << 20;
constexpr int L_R = 0, L_S0 = 48640, L_GS = 28800, L_UP = 14336 + 128;
constexpr int L_Q = 32768, L_V = 0, L_K = 49152;
constexpr int L_CS_BIG = 43008, L_ST = 46592, L_RS = 41984;
constexpr int LDS_BYTES = 163840;
constexpr int CODE_BYTES = 49152;
DI int kvaddr(int fr) { return ((fr & 2) ? L_V : L_K) + (fr >> 2) * 2048 + (fr & 1) * 1024; }
DI constexpr int gslot(int i) { return L_S0 + i * L_GS; }
DI constexpr int pslot(int s) { return (s % 10 < 5 ? s % 10 + 5 : s % 10 < 7 ? s % 10 - 2 : s % 10 - 7) * 16384; }

struct Params {
    const char* x; const char* wqkv; const char* wo; const char* wg; const char* wu; const char* wd;
    char* out; char* ws;
};

DI void glds(const char* sbase, unsigned voff, unsigned lds) {
    unsigned keep;
    asm volatile("s_mov_b32 %0, m0\n\ts_mov_b32 m0, %3\n\ts_nop 0\n\tglobal_load_lds_dwordx4 %1, %2\n\ts_mov_b32 m0, %0"
                 : "=&s"(keep) : "v"(voff), "s"(sbase), "s"(lds) : "memory");
}
DI void touch(const char* sbase, unsigned voff, unsigned lds) {
    unsigned keep;
    asm volatile("s_mov_b32 %0, m0\n\ts_mov_b32 m0, %3\n\ts_nop 0\n\tglobal_load_lds_dword %1, %2 sc1\n\ts_mov_b32 m0, %0"
                 : "=&s"(keep) : "v"(voff), "s"(sbase), "s"(lds) : "memory");
}
DI v4i16_t trr(lds_ptr p) { return __builtin_amdgcn_ds_read_tr16_b64_v4i16((LAS v4i16_t*)p); }
DI bf16x8 trfrag(lds_ptr p0, lds_ptr p1) {
    v4i16_t a = trr(p0), b = trr(p1);
    return __builtin_bit_cast(bf16x8, __builtin_shufflevector(a, b, 0, 1, 2, 3, 4, 5, 6, 7));
}
DI unsigned pk2(float a, float b) { f32x2 v = {a, b}; return __builtin_bit_cast(unsigned, __builtin_convertvector(v, bf16x2)); }
DI u32x4 pack8(const f32x16& c, int s) {
    u32x4 r = {pk2(c[8 * s], c[8 * s + 1]), pk2(c[8 * s + 2], c[8 * s + 3]), pk2(c[8 * s + 4], c[8 * s + 5]), pk2(c[8 * s + 6], c[8 * s + 7])};
    return r;
}
DI float bflo(unsigned u) { return __uint_as_float(u << 16); }
DI float bfhi(unsigned u) { return __uint_as_float(u & 0xffff0000u); }
DI float rbf(float a) { return bflo(pk2(a, a) & 0xffffu); }
DI float max3(float a, float b, float c) {
#pragma clang fp reassociate(on)
    return __builtin_amdgcn_fmed3f(__builtin_amdgcn_fmed3f(a, b, __builtin_inff()), c, __builtin_inff());
}
DI float xhalf(float v) {
    auto r = __builtin_amdgcn_permlane32_swap(__float_as_uint(v), __float_as_uint(v), false, false);
    return __uint_as_float((threadIdx.x & 32) ? r[0] : r[1]);
}
DI u32x4 ldws(__amdgpu_buffer_rsrc_t rs, unsigned off) { return __builtin_amdgcn_raw_buffer_load_b128(rs, (int)off, 0, AUX_SC1); }
DI void stws(__amdgpu_buffer_rsrc_t rs, unsigned off, u32x4 v) { __builtin_amdgcn_raw_buffer_store_b128(v, rs, (int)off, 0, AUX_SC1); }
DI void set_flag(char* ws, unsigned off, u64 tag, int lane) {
    (void)lane;
    __hip_atomic_store((gu64*)(ws + off), tag, RLX_AGENT);
}
DI void wait_flags(char* ws, unsigned off, u64 tag) {
    for (int spin = 0; spin < (1 << 20); ++spin) {
        u64 v = __hip_atomic_load((gu64*)(ws + off), RLX_AGENT);
        if (__all(v == tag)) break;
        __builtin_amdgcn_s_sleep(2);
    }
    __builtin_amdgcn_fence(__ATOMIC_ACQUIRE, "wavefront");
}
DI u64 poll_issue(char* ws, unsigned off) {
    u64 v;
    asm volatile("global_load_dwordx2 %0, %1, %2 sc1" : "=v"(v) : "v"(off), "s"(ws) : "memory");
    return v;
}
#define WAIT_SEL(c, NA, ...) asm volatile("s_cmp_lg_u32 %[sel], 0\n\ts_cbranch_scc1 1f\n\ts_waitcnt vmcnt(0)\n1:\n\ts_waitcnt vmcnt(" #NA ")" \
                                          : __VA_ARGS__ : [sel] "s"(c) : "scc", "memory")
#define POLL_CHECK(c, N, v, off) do { WAIT_SEL(c, N, "+v"(v)); if (!__all(v == tag)) wait_flags(p.ws, off, tag); } while (0)
DI u32x4 gran_issue(char* ws, unsigned off) {
    u32x4 v;
    asm volatile("global_load_dwordx4 %0, %1, %2 sc1" : "=v"(v) : "v"(off), "s"(ws) : "memory");
    return v;
}
DI bool gran_ok(u32x4 v, u64 tag) { return v[1] == (unsigned)tag && v[3] == (unsigned)(tag >> 32); }
DI unsigned gran_spin(__amdgpu_buffer_rsrc_t rs, unsigned off, u64 tag) {
    u32x4 v = ldws(rs, off);
    for (int spin = 0; spin < (1 << 20) && !__all(gran_ok(v, tag)); ++spin) { __builtin_amdgcn_s_sleep(2); v = ldws(rs, off); }
    return v[0];
}
#define STAMP(i)
#define STAMP_DECL
#define STAMP_OUT

#define SBAR() __builtin_amdgcn_sched_barrier(0)
#define RAWBAR() asm volatile("s_barrier" ::: "memory")
template <int V> struct IC { static constexpr int v = V; constexpr operator int() const { return V; } };
template <int I, int N, class F>
DI void sfor(F&& f) { if constexpr (I < N) { f(IC<I>{}); sfor<I + 1, N>(f); } }
template <int N, int D, class LD, class MM>
DI void pipe(LD ld, MM mm) {
    bf16x8 buf[D];
    sfor<0, D - 1>([&](auto i) { buf[i.v] = ld(i); });
    SBAR();
    sfor<0, N>([&](auto i) {
        if constexpr (i.v + D - 1 < N) buf[(i.v + D - 1) % D] = ld(IC<i.v + D - 1>{});
        SBAR();
        mm(i, buf[i.v % D]);
        SBAR();
    });
}
struct LB {
    unsigned b[3];
    DI LB(unsigned a) { b[0] = a; b[1] = a + 0xc000; b[2] = a + 0x18000 + 0x2000; asm volatile("" : "+v"(b[0]), "+v"(b[1]), "+v"(b[2])); }
    DI lds_ptr at(int off) const { return off < 0xc000 ? (lds_ptr)(size_t)b[0] + off : off < 0x1a000 ? (lds_ptr)(size_t)b[1] + (off - 0xc000) : (lds_ptr)(size_t)b[2] + (off - 0x1a000); }
};
DI float gelu_mul(float a, float b, float c1, float c2, float ir2) {
    float e = __builtin_amdgcn_exp2f(a * __builtin_fmaf(a * a, c2, c1));
    return a * b * __builtin_amdgcn_rcpf(__builtin_fmaf(e, ir2, ir2));
}

__global__ __launch_bounds__(512, 2) void fused_kernel(Params p) {
    extern __shared__ __attribute__((aligned(16))) char smem_[];
    lds_ptr sm = (lds_ptr)smem_;
    const unsigned smb = (unsigned)(size_t)sm;
    const int tid = threadIdx.x, lane = tid & 63;
    const int w = __builtin_amdgcn_readfirstlane(tid >> 6);
    const int hi = lane >> 5;
    const int xcd = blockIdx.x & 7, li = blockIdx.x >> 3;
    const int rt = 4 * (xcd >> 1) + (li >> 3), g = 8 * (xcd & 1) + (li & 7);
    const bool p3 = li < 28;
    const int rb = 4 * xcd + li / 7, cb = li % 7;
    const int rg = w & 3, cg = w >> 2;
    const int p3i = 28 - li > 0 ? 28 - li : 0;
    const __amdgpu_buffer_rsrc_t rs = __builtin_amdgcn_make_buffer_rsrc((void*)p.ws, 0, (int)WS_END, 0x00020000);
    STAMP_DECL; STAMP(0);

    const char* wq = p.wqkv + (56 * g - 8 * (g & 1));
    const char* wgb = p.wg + 224 * g;
    const char* wub = p.wu + 224 * g;
    u64 tag_ = poll_issue(p.ws, WS_EPOCH);
    {
        const char* xb = p.x + (size_t)(rt * 128 + w * 16) * 896;
        const unsigned xo = (lane >> 3) * 896 + ((lane & 7) ^ (lane >> 4)) * 16;
#pragma unroll
        for (int j = 0; j < 14; ++j)
            glds(xb + (j / 7) * 8 * 896 + (j % 7) * 128, j < 7 ? xo : xo ^ 64, smb + L_S0 + w * 14336 + j * 1024);
    }
    const unsigned qo = (lane >> 2) * 2688 + (lane & 3) * 16;
    auto issue_q = [&](int half, int pc) {
        if (w < 7) glds(wq + (pc >> 1) * 896 + (size_t)(half * 224 + (w * 2 + (pc & 1)) * 16) * 2688, qo,
                        smb + (half ? L_S0 : L_R) + (pc >> 1) * 14336 + (w * 2 + (pc & 1)) * 1024);
    };
#pragma unroll
    for (int j = 0; j < 6; ++j) issue_q(0, j);
    u32x4 cw[4];
    if (rt < 3 && w < 7) {
#pragma unroll
        for (int j = 0; j < 4; ++j)
            asm volatile("global_load_dwordx4 %0, %1, %2" : "=v"(cw[j]) : "v"(qo), "s"(wq + rt * 896 + (size_t)(w * 64 + j * 16) * 2688) : "memory");
    }
    const int ntw = 0;
    const int codew = li == 31 && w < CODE_BYTES / 8192;
    if (codew) {
        const char* pc;
        asm volatile("s_getpc_b64 %0" : "=s"(pc));
        touch(pc + w * 8192, lane * 128, smb + L_CS_BIG + w * 512);
    }
    unsigned goff[4];
    const int gp0 = cg ? 12 + rg * 4 : rg * 3;
#pragma unroll
    for (int j = 0; j < 4; ++j) {
        const int pc = gp0 + j, i = (pc % 14) * 64 + lane, row = i / 14, c = i - row * 14;
        goff[j] = row * 3584 + c * 16;
    }
    auto issue_g = [&](int st, int slot, int n) {
        const int pc = gp0 + n;
        if (n < 3 || cg) glds((pc >= 14 ? wub : wgb) + (size_t)st * 64 * 3584, goff[n], smb + slot + (pc >= 14 ? L_UP - 14336 : 0) + pc * 1024);
    };
    const unsigned poff = ((w & 1) * 16 + (lane >> 3)) * 896 + cb * 128 + (((lane & 7) ^ ((lane >> 2) & 4)) * 16);
    auto issue_p = [&](int st, int pc) {
        if (p3) {
            const int kq = w >> 1;
            const int hh = 4 * kq + st - 14;
            const char* b = st < 14 ? p.wd + (size_t)(448 * kq + 32 * st) * 896 : p.wo + (size_t)(28 * hh - 4 * (hh & 1)) * 896;
            glds(b + pc * 8 * 896, poff, smb + pslot(st) + w * 2048 + pc * 1024);
        }
    };
    u64 tag;
    auto cs_partial = [&]() {
        float cs[8];
#pragma unroll
        for (int e = 0; e < 4; ++e) {
            cs[2 * e] = (bflo(cw[0][e]) + bflo(cw[1][e])) + (bflo(cw[2][e]) + bflo(cw[3][e]));
            cs[2 * e + 1] = (bfhi(cw[0][e]) + bfhi(cw[1][e])) + (bfhi(cw[2][e]) + bfhi(cw[3][e]));
        }
#pragma unroll
        for (int e = 0; e < 8; ++e) {
            cs[e] += __builtin_bit_cast(float, __builtin_amdgcn_update_dpp(0, __builtin_bit_cast(int, cs[e]), 0x124, 0xf, 0xf, false));
            cs[e] += __builtin_bit_cast(float, __builtin_amdgcn_update_dpp(0, __builtin_bit_cast(int, cs[e]), 0x128, 0xf, 0xf, false));
        }
        {
            LAS f32x4* d = (LAS f32x4*)(sm + L_CS_BIG + w * 512 + (lane >> 4) * 128 + (lane & 3) * 32);
            d[0] = f32x4{cs[0], cs[1], cs[2], cs[3]}; d[1] = f32x4{cs[4], cs[5], cs[6], cs[7]};
        }
    };
    auto cs_publish = [&]() {
        float v = 0.f;
        LAS float* d = (LAS float*)(sm + L_CS_BIG + (lane & 31) * 4 + hi * 1792);
#pragma unroll
        for (int j = 0; j < 14; ++j) v += d[j * 32];
        v += xhalf(v);
        const unsigned h_ = pk2(v, v) & 0xffffu;
        const unsigned pv = h_ | (pk2(v - bflo(h_), 0.f) << 16);
        stws(rs, WS_CS + ((g * 10 + rt) * 32 + (lane & 31)) * 16, u32x4{pv, (unsigned)tag, pv, (unsigned)(tag >> 32)});
    };
    STAMP(1);
    {
        const int sel = w == 7 ? 0 : rt < 3 ? 10 : 6;
        asm volatile("s_cmp_eq_u32 %1, 10\n\ts_cbranch_scc1 1f\n\ts_cmp_eq_u32 %1, 6\n\ts_cbranch_scc1 2f\n\ts_waitcnt vmcnt(0)\n2:\n\ts_waitcnt vmcnt(6)\n1:\n\ts_waitcnt vmcnt(10)"
                     : "+v"(tag_) : "s"(sel) : "scc", "memory");
    }
    BAR();
    STAMP(2);
    tag = tag_ + 1;
    const int tile = rt * 4 + rg;
    const int q4 = (lane >> 2) & 3, p4 = lane & 3, g16 = (lane >> 4) & 1;
    const int kq = w >> 1, mh = w & 1, tt = p3 ? rb * 2 + mh : 0;
    const unsigned hfo = WS_HF + ((tt * 16 + 4 * kq) * 2 + (lane & 7)) * 64;
    const unsigned hb = WS_H + (tt * 112 + 28 * kq) * 1024 + lane * 16;
    u32x4 bf[28];
    auto phase1 = [&](auto cgc) {
    constexpr int cg = cgc.v;
    constexpr int NG = 3 + cg;
    auto issue_gc = [&](int st, int slot, int n) { if (n < NG) issue_g(st, slot, n); };
    bf16x8 xf[28];
    {
        const int t = lane & 31;
        lds_ptr xr = sm + L_S0 + (rg * 4 + (t >> 3)) * 7168 + (t & 7) * 128;
        const int sw = (t >> 1) & 7;
#pragma unroll
        for (int s_ = 0; s_ < 28; ++s_) { const int s = (s_ + 14 * cg) % 28, c = 2 * s + hi; xf[s] = *(LAS bf16x8*)(xr + (c >> 3) * 1024 + (((c ^ sw) & 7) * 16)); }
    }
    for (int j = 0; j < ntw; ++j) {
        int id = (xcd < 2 ? (li - 24) * 7 + j * 56 : li * 7) + w;
        id = id < 196 ? id : 195;
        const int u = (id % 98) * 64 + lane;
        touch((id < 98 ? p.wg : p.wu) + (xcd & 1) * 1792, (u / 14) * 3584 + (u % 14) * 128, smb + L_CS_BIG + w * 512);
    }
    const bool csd = rt >= 3 && rt < 10, csw = csd && w < 7;
    {
        float s1a[4] = {0.f, 0.f, 0.f, 0.f}, s2a[4] = {0.f, 0.f, 0.f, 0.f};
        const bf16x2 ones = {(__bf16)1.0f, (__bf16)1.0f};
        auto stat = [&](const bf16x8& xv) {
            u32x4 u = __builtin_bit_cast(u32x4, xv);
#pragma unroll
            for (int e = 0; e < 4; ++e) {
                const unsigned ue = u[e];
                const bf16x2 v = __builtin_bit_cast(bf16x2, ue);
                s1a[e] = __builtin_amdgcn_fdot2_f32_bf16(v, ones, s1a[e], false);
                s2a[e] = __builtin_amdgcn_fdot2_f32_bf16(v, v, s2a[e], false);
            }
        };
        if (cg == 0) {
#pragma unroll
            for (int s = 0; s < 14; ++s) stat(xf[s]);
        } else {
#pragma unroll
            for (int s = 14; s < 28; ++s) stat(xf[s]);
        }
        f32x2 sv = {(s1a[0] + s1a[1]) + (s1a[2] + s1a[3]), (s2a[0] + s2a[1]) + (s2a[2] + s2a[3])};
        sv[0] += xhalf(sv[0]); sv[1] += xhalf(sv[1]);
        STAMP(3);
        {
            const int nt_ = ntw + codew;
            if (nt_ == 0) WAITV(0); else if (nt_ == 1) WAITV(1); else if (nt_ == 2) WAITV(2); else WAITV(4);
        }
        *(LAS f32x2*)(sm + L_ST + w * 256 + (lane & 31) * 8) = sv;
    }
    if (rt < 3 && w < 7) { asm volatile("" : "+v"(cw[0]), "+v"(cw[1]), "+v"(cw[2]), "+v"(cw[3])); cs_partial(); }
    BAR();
    if (rt < 3 && w == 7) cs_publish();
    f32x16 acc[4];
    const float keeplo = ((g & 1) && hi == 0) ? 0.f : 1.f, keephi = (!(g & 1) && hi == 1) ? 0.f : 1.f;
    float rstd, ir2;
    bf16x8 mfr;
#pragma unroll
    for (int t = 0; t < 4; ++t) acc[t] = f32x16{};
    {
        STAMP(4);
        const unsigned cso = WS_CS + (g * 10 * 32 + (lane & 31)) * 16;
        u32x4 cg0v, cg1v;
        const LB qa(smb + (8 * hi + q4) * 64 + 32 * g16 + 8 * p4 + cg * 14336);
        auto qops = [&](int half, int n) {
            if (half == 0 && n < 6) issue_q(1, n);
            if (half == 0 && n >= 10) issue_gc(0, gslot(2), n - 10);
            if (half == 1 && n < 4) issue_gc(1, gslot(3), n);
            if (half == 1 && n == 6) { cg0v = gran_issue(p.ws, cso + cg * 512); cg1v = gran_issue(p.ws, cso + 1024); }
            if (half == 1 && n >= 8 && n < 12) issue_gc(2, L_R, n - 8);
        };
        sfor<0, 2>([&](auto hc) {
            constexpr int half = hc.v, base = half ? L_S0 : L_R;
            pipe<28, 5>([&](auto ic) { constexpr int i = ic.v;
                            bf16x8 a;
                            if ((i & 1) == 0 || cg) { lds_ptr r = qa.at(base + (i >> 1) * 1024 + (i & 1) * 14336); a = trfrag(r, r + 256); } else asm("" : "=v"(a));
                            return a;
                        },
                        [&](auto ic, bf16x8 a) { constexpr int i = ic.v;
                            if (i & 1) { if (cg) acc[1] = MFMA(xf[half * 14 + (i >> 1)], a, acc[1]); }
                            else { acc[0] = MFMA(a, xf[half * 14 + (i >> 1)], acc[0]); qops(half, i >> 1); }
                        });
            if (half == 0) {
                waitv<NG>();
                BAR();
                STAMP(5);
            }
        });
        STAMP(6);
        {
            const f32x2 sa = *(LAS f32x2*)(sm + L_ST + rg * 256 + (lane & 31) * 8), sb = *(LAS f32x2*)(sm + L_ST + (rg + 4) * 256 + (lane & 31) * 8);
            const float mean = (sa[0] + sb[0]) * (1.0f / 448.0f);
            ir2 = __builtin_fmaxf((sa[1] + sb[1]) * (1.0f / 448.0f) - mean * mean, 0.f) + 1e-5f;
            rstd = __builtin_amdgcn_rsqf(ir2);
            const float mh_ = -0.5f * mean;
            const unsigned m0_ = pk2(mh_, mh_), m1_ = pk2(mh_ - bflo(m0_ & 0xffffu), mh_ - bflo(m0_ & 0xffffu));
            mfr = __builtin_bit_cast(bf16x8, u32x4{m0_, m1_, 0u, 0u});
        }
        asm volatile("s_waitcnt vmcnt(%2)" : "+v"(cg0v), "+v"(cg1v) : "i"(NG) : "memory");
        RAWBAR();
        {
            unsigned c0 = cg0v[0], c1 = cg1v[0];
            if (!__all(gran_ok(cg0v, tag) && gran_ok(cg1v, tag))) { c0 = gran_spin(rs, cso + cg * 512, tag); c1 = gran_spin(rs, cso + 1024, tag); }
            acc[0] = MFMA(__builtin_bit_cast(bf16x8, u32x4{c0, c0, 0u, 0u}), mfr, acc[0]);
            if (cg) acc[1] = MFMA(mfr, __builtin_bit_cast(bf16x8, u32x4{c1, c1, 0u, 0u}), acc[1]);
        }
        const unsigned fo = WS_KV + (tile * 16 + g) * 4096 + lane * 16;
        if (cg == 0) {
            const float qs = 0.1889822365046136f * 1.4426950408889634f * rstd;
            const float qlo = qs * keeplo, qhi = qs * keephi;
#pragma unroll
            for (int r = 0; r < 16; ++r) acc[0][r] *= r < 4 ? qlo : r >= 12 ? qhi : qs;
            LAS u32x4* qd = (LAS u32x4*)(sm + L_Q + rg * 2048 + lane * 16);
            qd[0] = pack8(acc[0], 0); qd[64] = pack8(acc[0], 1);
        } else {
            const float vpad = ((g & 1) ? ((lane & 31) < 4) : ((lane & 31) >= 28)) ? 1.f : 0.f, vkeep = 1.f - vpad;
            LAS float* rt_ = (LAS float*)(sm + L_RS + rg * 128);
            rt_[lane & 31] = rstd;
#pragma unroll
            for (int c = 0; c < 4; ++c) {
                const f32x4 rr = *(LAS f32x4*)(rt_ + 8 * c + 4 * hi);
#pragma unroll
                for (int e = 0; e < 4; ++e) { acc[1][4 * c + e] = __builtin_fmaf(acc[1][4 * c + e] * vkeep, rr[e], vpad); acc[0][4 * c + e] *= rstd; }
            }
            stws(rs, fo, pack8(acc[0], 0)); stws(rs, fo + 1024, pack8(acc[0], 1));
            stws(rs, fo + 2048, pack8(acc[1], 0)); stws(rs, fo + 3072, pack8(acc[1], 1));
        }
        acc[0] = f32x16{}; acc[1] = f32x16{};
        STAMP(7);

        STAMP(8);
    }
    u32x4 kvr[6], csg[4];
    const unsigned csgo = WS_CS + ((g * 10 + 3 + 4 * cg) * 32 + (lane & 31)) * 16;
    const char* csb = ((lane & 2) ? wub : wgb) + (rt - 3) * 32 + (lane & 1) * 16 + (size_t)(w * 64 + (lane >> 2)) * 3584;
    u64 kvp;
    unsigned kvfo;
    { int kt = 4 * rt - 8 + ((6 * w) >> 2) + (lane & 1); kt = kt < 0 ? 0 : kt; kvfo = WS_KVF + (kt * 16 + g) * 64; }
    f32x16 o;
    float m;
    LAS f32x4* scr = (LAS f32x4*)(sm + (rg < 2 ? 24576 : 65536) + rg * 4096 + lane * 16);
    const bool padl = (g & 1) ? (hi == 0) : (hi == 1);
    {
        const LB ga(smb + (8 * hi + q4) * 224 + L_UP * g16 + 8 * p4 + cg * 128);
        auto ghook = [&](int st) {
            if (st == 0) STAMP(9); if (st == 1) STAMP(10); if (st == 4) STAMP(11); if (st == 6) STAMP(12);
            if (st == 0) {
                waitv<2 * NG + 4 * cg>();
            } else if (st == 1) { if (csw) waitv<NG + 3>(); else waitv<NG + 1>(); }
            else if (st == 2) {
                if (csw) waitv<2 * NG + 4>(); else waitv<2 * NG>();
                kvp = poll_issue(p.ws, kvfo);
            } else if (st == 3) {
                asm volatile("s_waitcnt vmcnt(0)" : "+v"(kvp) :: "memory");
                if (!__all(kvp == tag)) wait_flags(p.ws, kvfo, tag);
                if (csw) { asm volatile("" : "+v"(cw[0]), "+v"(cw[1]), "+v"(cw[2]), "+v"(cw[3])); cs_partial(); }
                BAR();
                if (csd && w == 7) cs_publish();
                return;
            }
            else if (st == 5) asm volatile("s_waitcnt lgkmcnt(0)" ::: "memory");
            RAWBAR();
        };
        auto gdma = [&](int st, int n) {
            if (st == 1 && n == 2 && cg) {
                waitv<10>();
                set_flag(p.ws, WS_KVF + (tile * 16 + g) * 64, tag, lane);
            }
            if (st == 0) { issue_gc(3, gslot(0), n); issue_gc(4, gslot(1), n); }
            if (st == 1 || st == 2) issue_gc(4 + st, gslot(1 + st), n);
            if ((st == 1 || st == 2) && n < 2 && csw)
                asm volatile("global_load_dwordx4 %0, %1, off" : "=v"(cw[2 * (st & 2) / 2 + n]) : "v"(csb + (size_t)(2 * (st & 2) / 2 + n) * 16 * 3584) : "memory");
            if (st == 4 && n < 3) {
#pragma unroll
                for (int e = 0; e < 2; ++e) {
                    const int fr = 6 * w + 2 * n + e;
                    int kt = 4 * rt - 8 + (fr >> 2); kt = kt < 0 ? 0 : kt;
                    kvr[2 * n + e] = ldws(rs, WS_KV + (kt * 16 + g) * 4096 + (fr & 3) * 1024 + lane * 16);
                }
            }
            if (st == 5 && n == 1) {
#pragma unroll
                for (int e = 0; e < 6; ++e) *(LAS u32x4*)(sm + kvaddr(6 * w + e) + lane * 16) = kvr[e];
            }
            if (st == 5 && n == 2) {
                const int wid = li * 8 + w;
                if (wid < 245) touch(wid < 196 ? p.wd + wid * 8192 : p.wo + (wid - 196) * 8192, lane * 128, smb + 40960);
            }
            if (st == 6 && n == 0) {
#pragma unroll
                for (int t = 0; t < 4 - cg; ++t) csg[t] = gran_issue(p.ws, csgo + t * 512);
            }
            if (st == 6 && n >= 1) { issue_p(n - 1, 0); issue_p(n - 1, 1); }
        };
        f32x16 sc[5];
        bf16x8 qf0, qf1, fa, fb;
        auto side = [&](auto kc) {
            constexpr int k = kc.v;
            auto kv = [&](int j, int part) { return *(LAS bf16x8*)(sm + kvaddr(4 * (rg + 5 * cg + (j < 4 ? j : 4 - cg)) + part) + lane * 16); };
            if constexpr (k == 0) {
                LAS bf16x8* ql_ = (LAS bf16x8*)(sm + L_Q + rg * 2048 + lane * 16);
                qf0 = ql_[0]; qf1 = ql_[64]; fa = kv(0, 0); fb = kv(0, 1); m = -3.0e38f;
            } else if constexpr (k <= 5) {
                constexpr int j = k - 1;
                const bf16x8 ka = fa, kb = fb;
                if constexpr (j < 4) { fa = kv(j + 1, 0); fb = kv(j + 1, 1); } else fa = kv(0, 2);
                f32x16 z = f32x16{};
                if constexpr (j == 0 || j == 3) {
                    const int ql = lane & 31;
                    const float big = ((j == 0) == (cg == 0)) ? 0x1p100f : 0.f;
                    const float b0 = (float)(j ? ql - 4 * hi : 4 * hi - ql - 1) * big;
#pragma unroll
                    for (int r = 0; r < 16; ++r) z[r] = __builtin_fminf(__builtin_fmaf(j ? -big : big, (float)((r & 3) + 8 * (r >> 2)), b0), 0.f);
                }
                if (j < 4 || cg == 0) { z = MFMA(ka, qf0, z); sc[j] = MFMA(kb, qf1, z); }
            } else if constexpr (k <= 10) {
                constexpr int j = k - 6;
                if (j < 4 || cg == 0) {
                    if (tile - (cg ? 3 : 8) + j < 0) {
                        asm volatile("");
#pragma unroll
                        for (int r = 0; r < 16; ++r) sc[j][r] = -1.0e30f;
                    }
#pragma unroll
                    for (int r = 0; r < 16; r += 2) m = max3(m, sc[j][r], sc[j][r + 1]);
                }
                if constexpr (j == 4) m = __builtin_fmaxf(m, xhalf(m));
            } else if constexpr (k <= 20) {
                constexpr int e = k - 11, j = e >> 1, h = e & 1;
                const bf16x8 va = fa;
                if constexpr (e < 9) fa = kv((e + 1) >> 1, 2 + ((e + 1) & 1));
                if (j < 4 || cg == 0) {
#pragma unroll
                    for (int r = 0; r < 8; ++r) sc[j][8 * h + r] = __builtin_amdgcn_exp2f(sc[j][8 * h + r] - m);
                    o = MFMA(va, __builtin_bit_cast(bf16x8, pack8(sc[j], h)), e == 0 ? f32x16{} : o);
                }
            } else if constexpr (k == 22) {
                if (cg == 0) {
                    if (g & 1) o[1] = padl ? m : o[1]; else o[13] = padl ? m : o[13];
#pragma unroll
                    for (int c = 0; c < 4; ++c) scr[c * 64] = f32x4{o[4 * c], o[4 * c + 1], o[4 * c + 2], o[4 * c + 3]};
                }
            }
        };
        auto gaddr = [&](int ks) {
            const int st = ks >> 2;
            const int slot = st == 0 ? gslot(2) : st == 1 ? gslot(3) : st == 2 ? L_R : st == 3 ? gslot(0) : st == 4 ? gslot(1) : st == 5 ? gslot(2) : gslot(3);
            return ga.at(slot + (ks & 3) * 3584);
        };
        pipe<112, 5>([&](auto ic) { constexpr int i = ic.v;
                         bf16x8 a;
                         if ((i & 3) < 3 || cg == 0) { lds_ptr r = gaddr(i >> 2) + 32 * (i & 3); a = trfrag(r, r + 896); } else asm("" : "=v"(a));
                         return a;
                     },
                     [&](auto ic, bf16x8 a) { constexpr int i = ic.v;
                         if ((i & 3) < 3 || cg == 0) acc[i & 3] = MFMA(a, xf[i >> 2], acc[i & 3]);
                         if ((i & 3) == 2 * cg) gdma(i >> 4, (i >> 2) & 3);
                         if ((i & 15) == 15) ghook(i >> 4);
                     });
    __builtin_amdgcn_s_setprio(1);
    if constexpr (cg == 0) WAIT_SEL(p3i, 6, "+v"(csg[0]), "+v"(csg[1]), "+v"(csg[2]), "+v"(csg[3]));
    else WAIT_SEL(p3i, 6, "+v"(csg[0]), "+v"(csg[1]), "+v"(csg[2]));
    {
        unsigned csv[4];
        bool ok = true;
#pragma unroll
        for (int t = 0; t < 4 - cg; ++t) { csv[t] = csg[t][0]; ok = ok && gran_ok(csg[t], tag); }
        if (!__all(ok)) {
#pragma unroll
            for (int t = 0; t < 4 - cg; ++t) csv[t] = gran_spin(rs, csgo + t * 512, tag);
        }
#pragma unroll
        for (int t = 0; t < 4 - cg; ++t) acc[t] = MFMA(__builtin_bit_cast(bf16x8, u32x4{csv[t], csv[t], 0u, 0u}), mfr, acc[t]);
    }
    STAMP(13);
    {
        const float gc1 = -2.0f * 0.7978845608028654f * 1.4426950408889634f * rstd, gc2 = gc1 * 0.044715f * rstd * rstd;
#pragma unroll
        for (int t = 0; t < 4; ++t) {
            if (t < 3 || cg == 0) {
                u32x4 hv;
#pragma unroll
                for (int e = 0; e < 4; ++e)
                    hv[e] = pk2(gelu_mul(acc[t][2 * e], acc[t][8 + 2 * e], gc1, gc2, ir2), gelu_mul(acc[t][2 * e + 1], acc[t][9 + 2 * e], gc1, gc2, ir2));
                stws(rs, WS_H + (tile * 112 + 7 * g + 4 * cg + t) * 1024 + lane * 16, hv);
                SBAR();
            }
        }
    }
    __builtin_amdgcn_s_setprio(0);
    STAMP(14);
    u64 hp;
    sfor<0, 23>([&](auto kc) {
        side(kc);
        if (kc.v == 6) {
            WAITV(0);
            set_flag(p.ws, WS_HF + ((tile * 16 + g) * 2 + cg) * 64, tag, lane);
            STAMP(15);
        }
        if (kc.v >= 7 && kc.v < 11) issue_p(3 + ((kc.v - 7) >> 1), (kc.v - 7) & 1);
        if (kc.v == 19) hp = poll_issue(p.ws, hfo);
        if (kc.v >= 6) SBAR();
    });
    STAMP(16);
    POLL_CHECK(p3i, 0, hp, hfo);
    STAMP(17);
    if (p3) {
#pragma unroll
        for (int i = 0; i < 8; ++i) bf[i] = ldws(rs, hb + i * 1024);
    }
    BAR();
    STAMP(18);
    if (cg == 1) {
        f32x16 oa;
#pragma unroll
        for (int c = 0; c < 4; ++c) { f32x4 v = scr[c * 64]; oa[4 * c] = v[0]; oa[4 * c + 1] = v[1]; oa[4 * c + 2] = v[2]; oa[4 * c + 3] = v[3]; }
        float ma = (g & 1) ? oa[1] : oa[13], la = (g & 1) ? oa[0] : oa[12], lb = (g & 1) ? o[0] : o[12];
        float ma2 = xhalf(ma), la2 = xhalf(la), lb2 = xhalf(lb);
        ma = padl ? ma : ma2; la = padl ? la : la2; lb = padl ? lb : lb2;
        const float mm = __builtin_fmaxf(ma, m);
        float wa = __builtin_amdgcn_exp2f(ma - mm), wb = __builtin_amdgcn_exp2f(m - mm);
        const float inv = __builtin_amdgcn_rcpf(la * wa + lb * wb);
        wa *= inv; wb *= inv;
#pragma unroll
        for (int r = 0; r < 16; ++r) {
            o[r] = oa[r] * wa + o[r] * wb;
            if (r < 4) o[r] *= keeplo;
            if (r >= 12) o[r] *= keephi;
        }
        const unsigned fo = WS_O + (tile * 16 + g) * 2048 + lane * 16;
        stws(rs, fo, pack8(o, 0)); stws(rs, fo + 1024, pack8(o, 1));
        if (!p3) { WAITV(0); set_flag(p.ws, WS_OF + (tile * 16 + g) * 64, tag, lane); }
    }
    }
    };
    if (cg == 0) phase1(IC<0>{}); else phase1(IC<1>{});
    STAMP(19);
    if (!p3) {
        if (blockIdx.x == 255 && w == 0) {
#pragma unroll
            for (int j = 0; j < 4; ++j) { int id = j * 64 + lane; wait_flags(p.ws, WS_KVF + ((id >> 4) * 64 + (id & 15)) * 64, tag); }
            set_flag(p.ws, WS_EPOCH, tag, lane);
        }
        STAMP_OUT;
        return;
    }
    {
        const unsigned ofo = WS_OF + (tt * 16 + 4 * kq + (lane & 3)) * 64;
        const unsigned ob = WS_O + (tt * 16 + 4 * kq) * 2048 + lane * 16;
        const u32x4 xres = *(const u32x4*)(p.x + (size_t)(rb * 64 + (tid >> 3)) * 896 + cb * 128 + (tid & 7) * 16);
        u64 op;
        f32x16 af[2], as[2];
        af[0] = f32x16{}; af[1] = f32x16{}; as[0] = f32x16{}; as[1] = f32x16{};
        const int lo = kq * 4096 + (4 * hi + q4) * 128 + 32 * g16 + 8 * p4;
        const int n0 = ((q4 >> 1) & 1) * 64;
        const LB pa(smb + lo + n0), pb(smb + lo + (64 - n0));
        auto fimg = [](int k) { return k == 0 ? 16384 : 64512 + k * 17408; };
        auto simg = [](int k) { return k == 0 ? 33792 : k == 1 ? 51200 : k == 2 ? 134144 : 0; };
        lds_ptr fd = sm + fimg(kq) + (mh * 32 + (lane & 31)) * 272 + hi * 16;
        auto phook = [&](int st) {
            if (st == 0) STAMP(21); if (st == 6) STAMP(22); if (st == 8) STAMP(23); if (st == 13) STAMP(24); if (st == 17) STAMP(25);
            if (st == 2 && cg) { WAITV(15); set_flag(p.ws, WS_OF + (tile * 16 + g) * 64, tag, lane); }
            if (st == 3 || st == 4) WAITV(15);
            else if (st == 5) { WAITV(15); op = poll_issue(p.ws, ofo); }
            else if (st == 6) WAITV(14);
            else if (st == 7) POLL_CHECK(p3i, 6, op, ofo);
            else if (st == 10 || st == 11) WAITV(12);
            else if (st == 9 || st == 12) WAITV(10); else if (st == 13) WAITV(6); else if (st == 14) WAITV(0);
            if (st < 15) RAWBAR();
        };
        pipe<72, 5>([&](auto ic) { constexpr int i = ic.v; lds_ptr r = ((i & 1) ? pb : pa).at(pslot(i >> 2) + ((i >> 1) & 1) * 2048); return trfrag(r, r + 1024); },
                    [&](auto ic, bf16x8 a) { constexpr int i = ic.v;
                        constexpr int ks = i >> 1, st = i >> 2, j = i & 3;
                        const bf16x8 b = __builtin_bit_cast(bf16x8, bf[ks % 28]);
                        if (ks < 28) af[i & 1] = MFMA(a, b, af[i & 1]); else as[i & 1] = MFMA(a, b, as[i & 1]);
                        if (st <= 12 && j < 2) issue_p(st + 5, j);
                        if constexpr (st < 6 && j >= 1) bf[7 + 3 * st + j] = ldws(rs, hb + (7 + 3 * st + j) * 1024);
                        if constexpr (st == 6 && (j & 1)) bf[14 + ks] = ldws(rs, hb + (14 + ks) * 1024);
                        if constexpr (st >= 8 && st <= 11 && (j & 1)) bf[ks - 16] = ldws(rs, ob + (ks - 16) * 1024);
                        if constexpr (st == 14 || st == 15) {
                            constexpr int nh = st - 14;
                            *(LAS f32x4*)(fd + nh * 128 + j * 32) = f32x4{af[nh][4 * j], af[nh][4 * j + 1], af[nh][4 * j + 2], af[nh][4 * j + 3]};
                        }
                        if (j == 3) phook(st);
                    });
        BAR();
        {
            lds_ptr d = sm + (kq < 3 ? simg(kq) + mh * 32 * 272 : mh ? 151552 : 0) + (lane & 31) * 272 + hi * 16;
#pragma unroll
            for (int nh = 0; nh < 2; ++nh)
#pragma unroll
                for (int c = 0; c < 4; ++c)
                    *(LAS f32x4*)(d + nh * 128 + c * 32) = f32x4{as[nh][4 * c], as[nh][4 * c + 1], as[nh][4 * c + 2], as[nh][4 * c + 3]};
        }
        BAR();
        {
            lds_ptr d = sm + (tid >> 3) * 272 + (tid & 7) * 32;
            f32x4 f0 = {}, f1 = {}, s0 = {}, s1 = {};
#pragma unroll
            for (int k = 0; k < 4; ++k) {
                const int so = k < 3 ? simg(k) : (w >= 4 ? 151552 - 32 * 272 : 0);
                f0 += *(LAS f32x4*)(d + fimg(k)); f1 += *(LAS f32x4*)(d + fimg(k) + 16);
                s0 += *(LAS f32x4*)(d + so); s1 += *(LAS f32x4*)(d + so + 16);
            }
            float r[8];
#pragma unroll
            for (int e = 0; e < 8; ++e) {
                const float f = e < 4 ? f0[e & 3] : f1[e & 3], s = e < 4 ? s0[e & 3] : s1[e & 3];
                const float gt = f * __builtin_amdgcn_rcpf(1.0f + __builtin_amdgcn_exp2f(-1.4426950408889634f * f));
                const float xv = (e & 1) ? bfhi(xres[e >> 1]) : bflo(xres[e >> 1]);
                r[e] = __builtin_fmaf(s, gt, xv);
            }
            u32x4 ov = {pk2(r[0], r[1]), pk2(r[2], r[3]), pk2(r[4], r[5]), pk2(r[6], r[7])};
            *(u32x4*)(p.out + (size_t)(rb * 64 + (tid >> 3)) * 896 + cb * 128 + (tid & 7) * 16) = ov;
            STAMP(26); STAMP_OUT;
        }
    }
}

extern "C" void kernel_launch(void* const* d_in, const int* in_sizes, int n_in, void* d_out, int out_size, void* d_ws, size_t ws_size,
                              hipStream_t stream) {
    (void)in_sizes; (void)n_in; (void)out_size; (void)ws_size;
    Params p;
    p.x = (const char*)d_in[0]; p.wqkv = (const char*)d_in[1]; p.wo = (const char*)d_in[2];
    p.wg = (const char*)d_in[4]; p.wu = (const char*)d_in[5]; p.wd = (const char*)d_in[6];
    p.out = (char*)d_out; p.ws = (char*)d_ws;
    (void)hipFuncSetAttribute((const void*)fused_kernel, hipFuncAttributeMaxDynamicSharedMemorySize, LDS_BYTES);
    fused_kernel<<<256, 512, LDS_BYTES, stream>>>(p);
}
```

```cpp
#include <hip/hip_runtime.h>
#include <hip/hip_bf16.h>

typedef __attribute__((ext_vector_type(8))) __bf16 bf16x8;
typedef __attribute__((ext_vector_type(2))) __bf16 bf16x2;
typedef __attribute__((ext_vector_type(16))) float f32x16;
typedef __attribute__((ext_vector_type(4))) float f32x4;
typedef __attribute__((ext_vector_type(2))) float f32x2;
typedef __attribute__((ext_vector_type(4))) unsigned u32x4;
typedef __attribute__((ext_vector_type(2))) unsigned u32x2;
typedef short v4i16_t __attribute__((ext_vector_type(4)));
typedef short v8i16_t __attribute__((ext_vector_type(8)));
typedef unsigned long long u64;
#define LAS __attribute__((address_space(3)))
typedef LAS char* lds_ptr;
typedef __attribute__((address_space(1))) u64 gu64;
#define DI __device__ __forceinline__
#define MFMA(a, b, c) __builtin_amdgcn_mfma_f32_32x32x16_bf16(a, b, c, 0, 0, 0)
#define RLX_AGENT __ATOMIC_RELAXED, __HIP_MEMORY_SCOPE_AGENT
#define WAITV(N) asm volatile("s_waitcnt vmcnt(" #N ")" ::: "memory")
#define BAR() asm volatile("s_waitcnt lgkmcnt(0)\n\ts_barrier" ::: "memory")
template <int N> DI void waitv() { asm volatile("s_waitcnt vmcnt(%0)" :: "i"(N) : "memory"); }

#define STAMPS 0

constexpr int AUX_SC1 = 16;
constexpr unsigned WS_EPOCH = 0;
constexpr unsigned WS_KVF = 4096;
constexpr unsigned WS_OF = WS_KVF + 65536;
constexpr unsigned WS_HF = WS_OF + 65536;
constexpr unsigned WS_CS = WS_HF + 131072;
constexpr unsigned WS_STAMP = 512 * 1024;
constexpr unsigned WS_KV = 1u << 20,
                    WS_O = 7u << 20, WS_H = 9u << 20, WS_END = 16u << 20;
constexpr int L_R = 0, L_S0 = 48640, L_GS = 28800, L_UP = 14336 + 128;
constexpr int L_Q = 32768, L_V = 0, L_K = 49152;
constexpr int L_CS_BIG = 43008, L_ST = 46592, L_RS = 41984;
constexpr int LDS_BYTES = 163840;
constexpr int CODE_BYTES = 49152;
DI int kvaddr(int fr) { return ((fr & 2) ? L_V : L_K) + (fr >> 2) * 2048 + (fr & 1) * 1024; }
DI constexpr int gslot(int i) { return L_S0 + i * L_GS; }
DI constexpr int pslot(int s) { return (s % 10 < 5 ? s % 10 + 5 : s % 10 < 7 ? s % 10 - 2 : s % 10 - 7) * 16384; }

struct Params {
    const char* x; const char* wqkv; const char* wo; const char* wg; const char* wu; const char* wd;
    char* out; char* ws;
};

DI void glds(const char* sbase, unsigned voff, unsigned lds) {
    unsigned keep;
    asm volatile("s_mov_b32 %0, m0\n\ts_mov_b32 m0, %3\n\ts_nop 0\n\tglobal_load_lds_dwordx4 %1, %2\n\ts_mov_b32 m0, %0"
                 : "=&s"(keep) : "v"(voff), "s"(sbase), "s"(lds) : "memory");
}
DI void touch(const char* sbase, unsigned voff, unsigned lds) {
    unsigned keep;
    asm volatile("s_mov_b32 %0, m0\n\ts_mov_b32 m0, %3\n\ts_nop 0\n\tglobal_load_lds_dword %1, %2 sc1\n\ts_mov_b32 m0, %0"
                 : "=&s"(keep) : "v"(voff), "s"(sbase), "s"(lds) : "memory");
}
DI v4i16_t trr(lds_ptr p) { return __builtin_amdgcn_ds_read_tr16_b64_v4i16((LAS v4i16_t*)p); }
DI bf16x8 trfrag(lds_ptr p0, lds_ptr p1) {
    v4i16_t a = trr(p0), b = trr(p1);
    return __builtin_bit_cast(bf16x8, __builtin_shufflevector(a, b, 0, 1, 2, 3, 4, 5, 6, 7));
}
DI unsigned pk2(float a, float b) { f32x2 v = {a, b}; return __builtin_bit_cast(unsigned, __builtin_convertvector(v, bf16x2)); }
DI u32x4 pack8(const f32x16& c, int s) {
    u32x4 r = {pk2(c[8 * s], c[8 * s + 1]), pk2(c[8 * s + 2], c[8 * s + 3]), pk2(c[8 * s + 4], c[8 * s + 5]), pk2(c[8 * s + 6], c[8 * s + 7])};
    return r;
}
DI float bflo(unsigned u) { return __uint_as_float(u << 16); }
DI float bfhi(unsigned u) { return __uint_as_float(u & 0xffff0000u); }
DI float max3(float a, float b, float c) {
#pragma clang fp reassociate(on)
    return __builtin_amdgcn_fmed3f(__builtin_amdgcn_fmed3f(a, b, __builtin_inff()), c, __builtin_inff());
}
DI float xhalf(float v) {
    auto r = __builtin_amdgcn_permlane32_swap(__float_as_uint(v), __float_as_uint(v), false, false);
    return __uint_as_float((threadIdx.x & 32) ? r[0] : r[1]);
}
DI u32x4 ldws(__amdgpu_buffer_rsrc_t rs, unsigned off) { return __builtin_amdgcn_raw_buffer_load_b128(rs, (int)off, 0, AUX_SC1); }
DI void stws(__amdgpu_buffer_rsrc_t rs, unsigned off, u32x4 v) { __builtin_amdgcn_raw_buffer_store_b128(v, rs, (int)off, 0, AUX_SC1); }
DI void set_flag(char* ws, unsigned off, u64 tag, int lane) {
    (void)lane;
    __hip_atomic_store((gu64*)(ws + off), tag, RLX_AGENT);
}
DI void wait_flags(char* ws, unsigned off, u64 tag) {
    for (int spin = 0; spin < (1 << 20); ++spin) {
        u64 v = __hip_atomic_load((gu64*)(ws + off), RLX_AGENT);
        if (__all(v == tag)) break;
        __builtin_amdgcn_s_sleep(2);
    }
    __builtin_amdgcn_fence(__ATOMIC_ACQUIRE, "wavefront");
}
DI u64 poll_issue(char* ws, unsigned off) {
    u64 v;
    asm volatile("global_load_dwordx2 %0, %1, %2 sc1" : "=v"(v) : "v"(off), "s"(ws) : "memory");
    return v;
}
#define WAIT_SEL(c, NA, ...) asm volatile("s_cmp_lg_u32 %[sel], 0\n\ts_cbranch_scc1 1f\n\ts_waitcnt vmcnt(0)\n1:\n\ts_waitcnt vmcnt(" #NA ")" \
                                          : __VA_ARGS__ : [sel] "s"(c) : "scc", "memory")
#define POLL_CHECK(c, N, v, off) do { WAIT_SEL(c, N, "+v"(v)); if (!__all(v == tag)) wait_flags(p.ws, off, tag); } while (0)
DI u32x4 gran_issue(char* ws, unsigned off) {
    u32x4 v;
    asm volatile("global_load_dwordx4 %0, %1, %2 sc1" : "=v"(v) : "v"(off), "s"(ws) : "memory");
    return v;
}
DI bool gran_ok(u32x4 v, u64 tag) { return v[1] == (unsigned)tag && v[3] == (unsigned)(tag >> 32); }
DI unsigned gran_spin(__amdgpu_buffer_rsrc_t rs, unsigned off, u64 tag) {
    u32x4 v = ldws(rs, off);
    for (int spin = 0; spin < (1 << 20) && !__all(gran_ok(v, tag)); ++spin) { __builtin_amdgcn_s_sleep(2); v = ldws(rs, off); }
    return v[0];
}
#define STAMP(i)
#define STAMP_DECL
#define STAMP_OUT

#define SBAR() __builtin_amdgcn_sched_barrier(0)
#define RAWBAR() asm volatile("s_barrier" ::: "memory")
template <int V> struct IC { static constexpr int v = V; constexpr operator int() const { return V; } };
template <int I, int N, class F>
DI void sfor(F&& f) { if constexpr (I < N) { f(IC<I>{}); sfor<I + 1, N>(f); } }
template <int N, int D, class LD, class MM>
DI void pipe(LD ld, MM mm) {
    bf16x8 buf[D];
    sfor<0, D - 1>([&](auto i) { buf[i.v] = ld(i); });
    SBAR();
    sfor<0, N>([&](auto i) {
        if constexpr (i.v + D - 1 < N) buf[(i.v + D - 1) % D] = ld(IC<i.v + D - 1>{});
        SBAR();
        mm(i, buf[i.v % D]);
        SBAR();
    });
}
struct LB {
    unsigned b[3];
    DI LB(unsigned a) { b[0] = a; b[1] = a + 0xc000; b[2] = a + 0x18000 + 0x2000; asm volatile("" : "+v"(b[0]), "+v"(b[1]), "+v"(b[2])); }
    DI lds_ptr at(int off) const { return off < 0xc000 ? (lds_ptr)(size_t)b[0] + off : off < 0x1a000 ? (lds_ptr)(size_t)b[1] + (off - 0xc000) : (lds_ptr)(size_t)b[2] + (off - 0x1a000); }
};
DI float gelu_mul(float a, float b, float c1, float c2, float ir2) {
    float e = __builtin_amdgcn_exp2f(a * __builtin_fmaf(a * a, c2, c1));
    return a * b * __builtin_amdgcn_rcpf(__builtin_fmaf(e, ir2, ir2));
}

__global__ __launch_bounds__(512, 2) void fused_kernel(Params p) {
    extern __shared__ __attribute__((aligned(16))) char smem_[];
    lds_ptr sm = (lds_ptr)smem_;
    const unsigned smb = (unsigned)(size_t)sm;
    const int tid = threadIdx.x, lane = tid & 63;
    const int w = __builtin_amdgcn_readfirstlane(tid >> 6);
    const int hi = lane >> 5;
    const int xcd = blockIdx.x & 7, li = blockIdx.x >> 3;
#define TW 84
#define XMAP 1
    const int rt = ((xcd >> (3 - XMAP)) << (4 - XMAP)) + (li >> (1 + XMAP)), g = ((xcd & ((1 << (3 - XMAP)) - 1)) << (1 + XMAP)) + (li & ((2 << XMAP) - 1));
    const bool p3 = li < 28;
    const int rb = 4 * xcd + li / 7, cb = li % 7;
    const int rg = w & 3, cg = w >> 2;
    const int p3i = 28 - li > 0 ? 28 - li : 0;
    const __amdgpu_buffer_rsrc_t rs = __builtin_amdgcn_make_buffer_rsrc((void*)p.ws, 0, (int)WS_END, 0x00020000);
    STAMP_DECL; STAMP(0);

    const char* wq = p.wqkv + (56 * g - 8 * (g & 1));
    const char* wgb = p.wg + 224 * g;
    const char* wub = p.wu + 224 * g;
    u64 tag_ = poll_issue(p.ws, WS_EPOCH);
    {
        const char* xb = p.x + (size_t)(rt * 128 + w * 16) * 896;
        const unsigned xo = (lane >> 3) * 896 + ((lane & 7) ^ (lane >> 4)) * 16;
#pragma unroll
        for (int j = 0; j < 14; ++j)
            glds(xb + (j / 7) * 8 * 896 + (j % 7) * 128, j < 7 ? xo : xo ^ 64, smb + L_S0 + w * 14336 + j * 1024);
    }
    const unsigned qo = (lane >> 2) * 2688 + (lane & 3) * 16;
    auto issue_q = [&](int half, int pc) {
        if (w < 7) glds(wq + (pc >> 1) * 896 + (size_t)(half * 224 + (w * 2 + (pc & 1)) * 16) * 2688, qo,
                        smb + (half ? L_S0 : L_R) + (pc >> 1) * 14336 + (w * 2 + (pc & 1)) * 1024);
    };
#pragma unroll
    for (int j = 0; j < 6; ++j) issue_q(0, j);
    u32x4 cw[4];
    if (rt < 3 && w < 7) {
#pragma unroll
        for (int j = 0; j < 4; ++j)
            asm volatile("global_load_dwordx4 %0, %1, %2" : "=v"(cw[j]) : "v"(qo), "s"(wq + rt * 896 + (size_t)(w * 64 + j * 16) * 2688) : "memory");
    }
    if (w == 7) {
        const int c = xcd & 3, l0 = c + (c >> 1);
        int u = li * 64 + lane; u = u < 2016 ? u : 2015;
        touch(p.wqkv + 224 * 2688, (u / 9) * 2688 + ((u % 9) / 3) * 896 + (l0 + u % 3) * 128, smb + 163328);
        const int id = li < 14 ? li : 13, v = (id % 7) * 64 + lane;
        touch((id < 7 ? p.wg : p.wu) + c * 896, (v / 7) * 3584 + (v % 7) * 128, smb + 163328);
    }
    const int codew = li == 31 && w < CODE_BYTES / 8192;
    if (codew) {
        const char* pc;
        asm volatile("s_getpc_b64 %0" : "=s"(pc));
        touch(pc + w * 8192, lane * 128, smb + L_CS_BIG + w * 512);
    }
    unsigned goff[4];
    const int gp0 = cg ? 12 + rg * 4 : rg * 3;
#pragma unroll
    for (int j = 0; j < 4; ++j) {
        const int pc = gp0 + j, i = (pc % 14) * 64 + lane, row = i / 14, c = i - row * 14;
        goff[j] = row * 3584 + c * 16;
    }
    auto issue_g = [&](int st, int slot, int n) {
        const int pc = gp0 + n;
        if (n < 3 || cg) glds((pc >= 14 ? wub : wgb) + (size_t)st * 64 * 3584, goff[n], smb + slot + (pc >= 14 ? L_UP - 14336 : 0) + pc * 1024);
    };
    const unsigned poff = ((w & 1) * 16 + (lane >> 3)) * 896 + cb * 128 + (((lane & 7) ^ ((lane >> 2) & 4)) * 16);
    auto issue_p = [&](int st, int pc) {
        if (p3) {
            const int kq = w >> 1;
            const int hh = 4 * kq + st - 14;
            const char* b = st < 14 ? p.wd + (size_t)(448 * kq + 32 * st) * 896 : p.wo + (size_t)(28 * hh - 4 * (hh & 1)) * 896;
            glds(b + pc * 8 * 896, poff, smb + pslot(st) + w * 2048 + pc * 1024);
        }
    };
    u64 tag;
    auto cs_partial = [&]() {
        float cs[8];
#pragma unroll
        for (int e = 0; e < 4; ++e) {
            cs[2 * e] = (bflo(cw[0][e]) + bflo(cw[1][e])) + (bflo(cw[2][e]) + bflo(cw[3][e]));
            cs[2 * e + 1] = (bfhi(cw[0][e]) + bfhi(cw[1][e])) + (bfhi(cw[2][e]) + bfhi(cw[3][e]));
        }
#pragma unroll
        for (int e = 0; e < 8; ++e) {
            cs[e] += __builtin_bit_cast(float, __builtin_amdgcn_update_dpp(0, __builtin_bit_cast(int, cs[e]), 0x124, 0xf, 0xf, false));
            cs[e] += __builtin_bit_cast(float, __builtin_amdgcn_update_dpp(0, __builtin_bit_cast(int, cs[e]), 0x128, 0xf, 0xf, false));
        }
        {
            LAS f32x4* d = (LAS f32x4*)(sm + L_CS_BIG + w * 512 + (lane >> 4) * 128 + (lane & 3) * 32);
            d[0] = f32x4{cs[0], cs[1], cs[2], cs[3]}; d[1] = f32x4{cs[4], cs[5], cs[6], cs[7]};
        }
    };
    auto cs_publish = [&]() {
        float v = 0.f;
        LAS float* d = (LAS float*)(sm + L_CS_BIG + (lane & 31) * 4 + hi * 1792);
#pragma unroll
        for (int j = 0; j < 14; ++j) v += d[j * 32];
        v += xhalf(v);
        const unsigned h_ = pk2(v, v) & 0xffffu;
        const unsigned pv = h_ | (pk2(v - bflo(h_), 0.f) << 16);
        stws(rs, WS_CS + ((g * 10 + rt) * 32 + (lane & 31)) * 16, u32x4{pv, (unsigned)tag, pv, (unsigned)(tag >> 32)});
    };
    STAMP(1);
    {
        const int sel = w == 7 ? 0 : rt < 3 ? 10 : 6;
        asm volatile("s_cmp_eq_u32 %1, 10\n\ts_cbranch_scc1 1f\n\ts_cmp_eq_u32 %1, 6\n\ts_cbranch_scc1 2f\n\ts_waitcnt vmcnt(2)\n2:\n\ts_waitcnt vmcnt(6)\n1:\n\ts_waitcnt vmcnt(10)"
                     : "+v"(tag_) : "s"(sel) : "scc", "memory");
    }
    BAR();
    STAMP(2);
    tag = tag_ + 1;
    const int tile = rt * 4 + rg;
    const int q4 = (lane >> 2) & 3, p4 = lane & 3, g16 = (lane >> 4) & 1;
    const int kq = w >> 1, mh = w & 1, tt = p3 ? rb * 2 + mh : 0;
    const unsigned hfo = WS_HF + ((tt * 16 + 4 * kq) * 2 + (lane & 7)) * 64;
    const unsigned hb = WS_H + (tt * 112 + 28 * kq) * 1024 + lane * 16;
    u32x4 bf[28];
    auto phase1 = [&](auto cgc) {
    constexpr int cg = cgc.v;
    constexpr int NG = 3 + cg;
    auto issue_gc = [&](int st, int slot, int n) { if (n < NG) issue_g(st, slot, n); };
    u32x4 q1r[6];
    {
        const int wr = w < 7 ? w : 6;
#pragma unroll
        for (int j = 0; j < 6; ++j) q1r[j] = *(const u32x4*)(wq + (j >> 1) * 896 + (size_t)(224 + (wr * 2 + (j & 1)) * 16) * 2688 + qo);
    }
    bf16x8 xf[28];
    {
        const int t = lane & 31;
        lds_ptr xr = sm + L_S0 + (rg * 4 + (t >> 3)) * 7168 + (t & 7) * 128;
        const int sw = (t >> 1) & 7;
#pragma unroll
        for (int s_ = 0; s_ < 28; ++s_) { const int s = (s_ + 14 * cg) % 28, c = 2 * s + hi; xf[s] = *(LAS bf16x8*)(xr + (c >> 3) * 1024 + (((c ^ sw) & 7) * 16)); }
    }
    const bool csd = rt >= 3 && rt < 10, csw = csd && w < 7;
    {
        float s1a[4] = {0.f, 0.f, 0.f, 0.f}, s2a[4] = {0.f, 0.f, 0.f, 0.f};
        const bf16x2 ones = {(__bf16)1.0f, (__bf16)1.0f};
        auto stat = [&](const bf16x8& xv) {
            u32x4 u = __builtin_bit_cast(u32x4, xv);
#pragma unroll
            for (int e = 0; e < 4; ++e) {
                const unsigned ue = u[e];
                const bf16x2 v = __builtin_bit_cast(bf16x2, ue);
                s1a[e] = __builtin_amdgcn_fdot2_f32_bf16(v, ones, s1a[e], false);
                s2a[e] = __builtin_amdgcn_fdot2_f32_bf16(v, v, s2a[e], false);
            }
        };
        if (cg == 0) {
#pragma unroll
            for (int s = 0; s < 14; ++s) stat(xf[s]);
        } else {
#pragma unroll
            for (int s = 14; s < 28; ++s) stat(xf[s]);
        }
        f32x2 sv = {(s1a[0] + s1a[1]) + (s1a[2] + s1a[3]), (s2a[0] + s2a[1]) + (s2a[2] + s2a[3])};
        sv[0] += xhalf(sv[0]); sv[1] += xhalf(sv[1]);
        STAMP(3);
        WAITV(0);
        *(LAS f32x2*)(sm + L_ST + w * 256 + (lane & 31) * 8) = sv;
    }
    if (rt < 3 && w < 7) { asm volatile("" : "+v"(cw[0]), "+v"(cw[1]), "+v"(cw[2]), "+v"(cw[3])); cs_partial(); }
    BAR();
    if (w < 7) {
#pragma unroll
        for (int j = 0; j < 6; ++j) *(LAS u32x4*)(sm + L_S0 + (j >> 1) * 14336 + (w * 2 + (j & 1)) * 1024 + lane * 16) = q1r[j];
    }
    if (rt < 3 && w == 7) cs_publish();
    f32x16 acc[4];
    const float keeplo = ((g & 1) && hi == 0) ? 0.f : 1.f, keephi = (!(g & 1) && hi == 1) ? 0.f : 1.f;
    float rstd, ir2;
    bf16x8 mfr;
#pragma unroll
    for (int t = 0; t < 4; ++t) acc[t] = f32x16{};
    {
        STAMP(4);
        const unsigned cso = WS_CS + (g * 10 * 32 + (lane & 31)) * 16;
        u32x4 cg0v, cg1v;
        const LB qa(smb + (8 * hi + q4) * 64 + 32 * g16 + 8 * p4 + cg * 14336);
        auto qops = [&](int half, int n) {
            if (half == 0 && n >= 1 && n < 12 && n % 6 < 5 && n % 6 > 0) issue_gc(n / 6, gslot(2 + n / 6), n % 6 - 1);
            if (half == 1 && n == 2) { cg0v = gran_issue(p.ws, cso + cg * 512); cg1v = gran_issue(p.ws, cso + 1024); }
            if (half == 1 && n >= 4 && n < 12 && !(n & 1)) issue_gc(2, L_R, (n - 4) >> 1);
        };
        sfor<0, 2>([&](auto hc) {
            constexpr int half = hc.v, base = half ? L_S0 : L_R;
            pipe<28, 5>([&](auto ic) { constexpr int i = ic.v;
                            bf16x8 a;
                            if ((i & 1) == 0 || cg) { lds_ptr r = qa.at(base + (i >> 1) * 1024 + (i & 1) * 14336); a = trfrag(r, r + 256); } else asm("" : "=v"(a));
                            return a;
                        },
                        [&](auto ic, bf16x8 a) { constexpr int i = ic.v;
                            if (i & 1) { if (cg) acc[1] = MFMA(xf[half * 14 + (i >> 1)], a, acc[1]); }
                            else { acc[0] = MFMA(a, xf[half * 14 + (i >> 1)], acc[0]); qops(half, i >> 1); }
                        });
            if (half == 0) {
                const int id = li * 4 + rg;
                if (cg == 0 && XMAP == 1 && id < TW) {
                    const int u = (id % (TW / 2)) * 64 + lane;
                    touch((id < TW / 2 ? p.wg : p.wu) + (xcd & 3) * 896 + (size_t)(448 - TW * 32 / 7) * 3584, (u / 7) * 3584 + (u % 7) * 128, smb + 92160 + w * 256);
                }
                BAR();
                STAMP(5);
            }
        });
        STAMP(6);
        {
            const f32x2 sa = *(LAS f32x2*)(sm + L_ST + rg * 256 + (lane & 31) * 8), sb = *(LAS f32x2*)(sm + L_ST + (rg + 4) * 256 + (lane & 31) * 8);
            const float mean = (sa[0] + sb[0]) * (1.0f / 448.0f);
            ir2 = __builtin_fmaxf((sa[1] + sb[1]) * (1.0f / 448.0f) - mean * mean, 0.f) + 1e-5f;
            rstd = __builtin_amdgcn_rsqf(ir2);
            const float mh_ = -0.5f * mean;
            const unsigned m0_ = pk2(mh_, mh_), m1_ = pk2(mh_ - bflo(m0_ & 0xffffu), mh_ - bflo(m0_ & 0xffffu));
            mfr = __builtin_bit_cast(bf16x8, u32x4{m0_, m1_, 0u, 0u});
        }
        asm volatile("s_waitcnt vmcnt(%2)" : "+v"(cg0v), "+v"(cg1v) : "i"(NG) : "memory");
        RAWBAR();
        {
            unsigned c0 = cg0v[0], c1 = cg1v[0];
            if (!__all(gran_ok(cg0v, tag) && gran_ok(cg1v, tag))) { c0 = gran_spin(rs, cso + cg * 512, tag); c1 = gran_spin(rs, cso + 1024, tag); }
            acc[0] = MFMA(__builtin_bit_cast(bf16x8, u32x4{c0, c0, 0u, 0u}), mfr, acc[0]);
            if (cg) acc[1] = MFMA(mfr, __builtin_bit_cast(bf16x8, u32x4{c1, c1, 0u, 0u}), acc[1]);
        }
        const unsigned fo = WS_KV + (tile * 16 + g) * 4096 + lane * 16;
        if (cg == 0) {
            const float qs = 0.1889822365046136f * 1.4426950408889634f * rstd;
            const float qlo = qs * keeplo, qhi = qs * keephi;
#pragma unroll
            for (int r = 0; r < 16; ++r) acc[0][r] *= r < 4 ? qlo : r >= 12 ? qhi : qs;
            LAS u32x4* qd = (LAS u32x4*)(sm + L_Q + rg * 2048 + lane * 16);
            qd[0] = pack8(acc[0], 0); qd[64] = pack8(acc[0], 1);
        } else {
            const float vpad = ((g & 1) ? ((lane & 31) < 4) : ((lane & 31) >= 28)) ? 1.f : 0.f, vkeep = 1.f - vpad;
            LAS float* rt_ = (LAS float*)(sm + L_RS + rg * 128);
            rt_[lane & 31] = rstd;
#pragma unroll
            for (int c = 0; c < 4; ++c) {
                const f32x4 rr = *(LAS f32x4*)(rt_ + 8 * c + 4 * hi);
#pragma unroll
                for (int e = 0; e < 4; ++e) { acc[1][4 * c + e] = __builtin_fmaf(acc[1][4 * c + e] * vkeep, rr[e], vpad); acc[0][4 * c + e] *= rstd; }
            }
            stws(rs, fo, pack8(acc[0], 0)); stws(rs, fo + 1024, pack8(acc[0], 1));
            stws(rs, fo + 2048, pack8(acc[1], 0)); stws(rs, fo + 3072, pack8(acc[1], 1));
        }
        acc[0] = f32x16{}; acc[1] = f32x16{};
        STAMP(7);

        STAMP(8);
    }
    u32x4 kvr[6], csg[4];
    const unsigned csgo = WS_CS + ((g * 10 + 3 + 4 * cg) * 32 + (lane & 31)) * 16;
    const char* csb = ((lane & 2) ? wub : wgb) + (rt - 3) * 32 + (lane & 1) * 16 + (size_t)(w * 64 + (lane >> 2)) * 3584;
    u64 kvp;
    unsigned kvfo;
    { int kt = 4 * rt - 8 + ((6 * w) >> 2) + (lane & 1); kt = kt < 0 ? 0 : kt; kvfo = WS_KVF + (kt * 16 + g) * 64; }
    f32x16 o;
    float m;
    LAS f32x4* scr = (LAS f32x4*)(sm + (rg < 2 ? 24576 : 65536) + rg * 4096 + lane * 16);
    const bool padl = (g & 1) ? (hi == 0) : (hi == 1);
    {
        const LB ga(smb + (8 * hi + q4) * 224 + L_UP * g16 + 8 * p4 + cg * 128);
        auto ghook = [&](int st) {
            if (st == 0) STAMP(9); if (st == 1) STAMP(10); if (st == 4) STAMP(11); if (st == 6) STAMP(12);
            if (st == 0) {
                waitv<2 * NG + 4 * cg>();
            } else if (st == 1) { if (csw) waitv<NG + 3>(); else waitv<NG + 1>(); }
            else if (st == 2) {
                if (csw) waitv<2 * NG + 4>(); else waitv<2 * NG>();
                kvp = poll_issue(p.ws, kvfo);
            } else if (st == 3) {
                asm volatile("s_waitcnt vmcnt(0)" : "+v"(kvp) :: "memory");
                if (!__all(kvp == tag)) wait_flags(p.ws, kvfo, tag);
                if (csw) { asm volatile("" : "+v"(cw[0]), "+v"(cw[1]), "+v"(cw[2]), "+v"(cw[3])); cs_partial(); }
                BAR();
                if (csd && w == 7) cs_publish();
                return;
            }
            else if (st == 5) asm volatile("s_waitcnt lgkmcnt(0)" ::: "memory");
            RAWBAR();
        };
        auto gdma = [&](int st, int n) {
            if (st == 1 && n == 2 && cg) {
                waitv<10>();
                set_flag(p.ws, WS_KVF + (tile * 16 + g) * 64, tag, lane);
            }
            if (st == 0) { issue_gc(3, gslot(0), n); issue_gc(4, gslot(1), n); }
            if (st == 1 || st == 2) issue_gc(4 + st, gslot(1 + st), n);
            if ((st == 1 || st == 2) && n < 2 && csw)
                asm volatile("global_load_dwordx4 %0, %1, off" : "=v"(cw[2 * (st & 2) / 2 + n]) : "v"(csb + (size_t)(2 * (st & 2) / 2 + n) * 16 * 3584) : "memory");
            if (st == 4 && n < 3) {
#pragma unroll
                for (int e = 0; e < 2; ++e) {
                    const int fr = 6 * w + 2 * n + e;
                    int kt = 4 * rt - 8 + (fr >> 2); kt = kt < 0 ? 0 : kt;
                    kvr[2 * n + e] = ldws(rs, WS_KV + (kt * 16 + g) * 4096 + (fr & 3) * 1024 + lane * 16);
                }
            }
            if (st == 5 && n == 1) {
#pragma unroll
                for (int e = 0; e < 6; ++e) *(LAS u32x4*)(sm + kvaddr(6 * w + e) + lane * 16) = kvr[e];
            }
            if (st == 5 && n >= 2) issue_p(0, n - 2);
            if (st == 6 && n == 0) {
#pragma unroll
                for (int t = 0; t < 4 - cg; ++t) csg[t] = gran_issue(p.ws, csgo + t * 512);
            }
            if (st == 6 && (n == 1 || n == 2)) { issue_p(n, 0); issue_p(n, 1); }
            if (st == 6 && n == 3 && p3) {
                const int id = li * 8 + w;
                touch(id < 196 ? p.wd + id * 8192 : p.wo + (id - 196) * 8192, lane * 128, smb + 40960);
            }
        };
        f32x16 sc[5];
        bf16x8 qf0, qf1, fa, fb;
        auto side = [&](auto kc) {
            constexpr int k = kc.v;
            auto kv = [&](int j, int part) { return *(LAS bf16x8*)(sm + kvaddr(4 * (rg + 5 * cg + (j < 4 ? j : 4 - cg)) + part) + lane * 16); };
            if constexpr (k == 0) {
                LAS bf16x8* ql_ = (LAS bf16x8*)(sm + L_Q + rg * 2048 + lane * 16);
                qf0 = ql_[0]; qf1 = ql_[64]; fa = kv(0, 0); fb = kv(0, 1); m = -3.0e38f;
            } else if constexpr (k <= 5) {
                constexpr int j = k - 1;
                const bf16x8 ka = fa, kb = fb;
                if constexpr (j < 4) { fa = kv(j + 1, 0); fb = kv(j + 1, 1); } else fa = kv(0, 2);
                f32x16 z = f32x16{};
                if constexpr (j == 0 || j == 3) {
                    const int ql = lane & 31;
                    const float big = ((j == 0) == (cg == 0)) ? 0x1p100f : 0.f;
                    const float b0 = (float)(j ? ql - 4 * hi : 4 * hi - ql - 1) * big;
#pragma unroll
                    for (int r = 0; r < 16; ++r) z[r] = __builtin_fminf(__builtin_fmaf(j ? -big : big, (float)((r & 3) + 8 * (r >> 2)), b0), 0.f);
                }
                if (j < 4 || cg == 0) { z = MFMA(ka, qf0, z); sc[j] = MFMA(kb, qf1, z); }
            } else if constexpr (k <= 10) {
                constexpr int j = k - 6;
                if (j < 4 || cg == 0) {
                    if (tile - (cg ? 3 : 8) + j < 0) {
                        asm volatile("");
#pragma unroll
                        for (int r = 0; r < 16; ++r) sc[j][r] = -1.0e30f;
                    }
#pragma unroll
                    for (int r = 0; r < 16; r += 2) m = max3(m, sc[j][r], sc[j][r + 1]);
                }
                if constexpr (j == 4) m = __builtin_fmaxf(m, xhalf(m));
            } else if constexpr (k <= 20) {
                constexpr int e = k - 11, j = e >> 1, h = e & 1;
                const bf16x8 va = fa;
                if constexpr (e < 9) fa = kv((e + 1) >> 1, 2 + ((e + 1) & 1));
                if (j < 4 || cg == 0) {
#pragma unroll
                    for (int r = 0; r < 8; ++r) sc[j][8 * h + r] = __builtin_amdgcn_exp2f(sc[j][8 * h + r] - m);
                    o = MFMA(va, __builtin_bit_cast(bf16x8, pack8(sc[j], h)), e == 0 ? f32x16{} : o);
                }
            } else if constexpr (k == 22) {
                if (cg == 0) {
                    if (g & 1) o[1] = padl ? m : o[1]; else o[13] = padl ? m : o[13];
#pragma unroll
                    for (int c = 0; c < 4; ++c) scr[c * 64] = f32x4{o[4 * c], o[4 * c + 1], o[4 * c + 2], o[4 * c + 3]};
                }
            }
        };
        auto gaddr = [&](int ks) {
            const int st = ks >> 2;
            const int slot = st == 0 ? gslot(2) : st == 1 ? gslot(3) : st == 2 ? L_R : st == 3 ? gslot(0) : st == 4 ? gslot(1) : st == 5 ? gslot(2) : gslot(3);
            return ga.at(slot + (ks & 3) * 3584);
        };
        pipe<112, 5>([&](auto ic) { constexpr int i = ic.v;
                         bf16x8 a;
                         if ((i & 3) < 3 || cg == 0) { lds_ptr r = gaddr(i >> 2) + 32 * (i & 3); a = trfrag(r, r + 896); } else asm("" : "=v"(a));
                         return a;
                     },
                     [&](auto ic, bf16x8 a) { constexpr int i = ic.v;
                         if ((i & 3) < 3 || cg == 0) acc[i & 3] = MFMA(a, xf[i >> 2], acc[i & 3]);
                         if ((i & 3) == 2 * cg) gdma(i >> 4, (i >> 2) & 3);
                         if ((i & 15) == 15) ghook(i >> 4);
                     });
    __builtin_amdgcn_s_setprio(1);
    if constexpr (cg == 0) WAIT_SEL(p3i, 5, "+v"(csg[0]), "+v"(csg[1]), "+v"(csg[2]), "+v"(csg[3]));
    else WAIT_SEL(p3i, 5, "+v"(csg[0]), "+v"(csg[1]), "+v"(csg[2]));
    {
        unsigned csv[4];
        bool ok = true;
#pragma unroll
        for (int t = 0; t < 4 - cg; ++t) { csv[t] = csg[t][0]; ok = ok && gran_ok(csg[t], tag); }
        if (!__all(ok)) {
#pragma unroll
            for (int t = 0; t < 4 - cg; ++t) csv[t] = gran_spin(rs, csgo + t * 512, tag);
        }
#pragma unroll
        for (int t = 0; t < 4 - cg; ++t) acc[t] = MFMA(__builtin_bit_cast(bf16x8, u32x4{csv[t], csv[t], 0u, 0u}), mfr, acc[t]);
    }
    STAMP(13);
    {
        const float gc1 = -2.0f * 0.7978845608028654f * 1.4426950408889634f * rstd, gc2 = gc1 * 0.044715f * rstd * rstd;
#pragma unroll
        for (int t = 0; t < 4; ++t) {
            if (t < 3 || cg == 0) {
                u32x4 hv;
#pragma unroll
                for (int e = 0; e < 4; ++e)
                    hv[e] = pk2(gelu_mul(acc[t][2 * e], acc[t][8 + 2 * e], gc1, gc2, ir2), gelu_mul(acc[t][2 * e + 1], acc[t][9 + 2 * e], gc1, gc2, ir2));
                stws(rs, WS_H + (tile * 112 + 7 * g + 4 * cg + t) * 1024 + lane * 16, hv);
                SBAR();
            }
        }
    }
    __builtin_amdgcn_s_setprio(0);
    STAMP(14);
    u64 hp;
    sfor<0, 23>([&](auto kc) {
        side(kc);
        if (kc.v == 6) {
            WAITV(0);
            set_flag(p.ws, WS_HF + ((tile * 16 + g) * 2 + cg) * 64, tag, lane);
            STAMP(15);
        }
        if (kc.v >= 7 && kc.v < 11) issue_p(3 + ((kc.v - 7) >> 1), (kc.v - 7) & 1);
        if (kc.v == 19) hp = poll_issue(p.ws, hfo);
        if (kc.v >= 6) SBAR();
    });
    STAMP(16);
    POLL_CHECK(p3i, 0, hp, hfo);
    STAMP(17);
    if (p3) {
#pragma unroll
        for (int i = 0; i < 8; ++i) bf[i] = ldws(rs, hb + i * 1024);
    }
    BAR();
    STAMP(18);
    if (cg == 1) {
        f32x16 oa;
#pragma unroll
        for (int c = 0; c < 4; ++c) { f32x4 v = scr[c * 64]; oa[4 * c] = v[0]; oa[4 * c + 1] = v[1]; oa[4 * c + 2] = v[2]; oa[4 * c + 3] = v[3]; }
        float ma = (g & 1) ? oa[1] : oa[13], la = (g & 1) ? oa[0] : oa[12], lb = (g & 1) ? o[0] : o[12];
        float ma2 = xhalf(ma), la2 = xhalf(la), lb2 = xhalf(lb);
        ma = padl ? ma : ma2; la = padl ? la : la2; lb = padl ? lb : lb2;
        const float mm = __builtin_fmaxf(ma, m);
        float wa = __builtin_amdgcn_exp2f(ma - mm), wb = __builtin_amdgcn_exp2f(m - mm);
        const float inv = __builtin_amdgcn_rcpf(la * wa + lb * wb);
        wa *= inv; wb *= inv;
#pragma unroll
        for (int r = 0; r < 16; ++r) {
            o[r] = oa[r] * wa + o[r] * wb;
            if (r < 4) o[r] *= keeplo;
            if (r >= 12) o[r] *= keephi;
        }
        const unsigned fo = WS_O + (tile * 16 + g) * 2048 + lane * 16;
        stws(rs, fo, pack8(o, 0)); stws(rs, fo + 1024, pack8(o, 1));
        if (!p3) { WAITV(0); set_flag(p.ws, WS_OF + (tile * 16 + g) * 64, tag, lane); }
    }
    }
    };
    if (cg == 0) phase1(IC<0>{}); else phase1(IC<1>{});
    STAMP(19);
    if (!p3) {
        if (blockIdx.x == 255 && w == 0) {
#pragma unroll
            for (int j = 0; j < 4; ++j) { int id = j * 64 + lane; wait_flags(p.ws, WS_KVF + ((id >> 4) * 64 + (id & 15)) * 64, tag); }
            set_flag(p.ws, WS_EPOCH, tag, lane);
        }
        STAMP_OUT;
        return;
    }
    {
        const unsigned ofo = WS_OF + (tt * 16 + 4 * kq + (lane & 3)) * 64;
        const unsigned ob = WS_O + (tt * 16 + 4 * kq) * 2048 + lane * 16;
        const u32x4 xres = *(const u32x4*)(p.x + (size_t)(rb * 64 + (tid >> 3)) * 896 + cb * 128 + (tid & 7) * 16);
        u64 op;
        f32x16 af[2], as[2];
        af[0] = f32x16{}; af[1] = f32x16{}; as[0] = f32x16{}; as[1] = f32x16{};
        const int lo = kq * 4096 + (4 * hi + q4) * 128 + 32 * g16 + 8 * p4;
        const int n0 = ((q4 >> 1) & 1) * 64;
        const LB pa(smb + lo + n0), pb(smb + lo + (64 - n0));
        auto fimg = [](int k) { return k == 0 ? 16384 : 64512 + k * 17408; };
        auto simg = [](int k) { return k == 0 ? 33792 : k == 1 ? 51200 : k == 2 ? 134144 : 0; };
        lds_ptr fd = sm + fimg(kq) + (mh * 32 + (lane & 31)) * 272 + hi * 16;
        auto phook = [&](int st) {
            if (st == 0) STAMP(21); if (st == 6) STAMP(22); if (st == 8) STAMP(23); if (st == 13) STAMP(24); if (st == 17) STAMP(25);
            if (st == 2 && cg) { WAITV(15); set_flag(p.ws, WS_OF + (tile * 16 + g) * 64, tag, lane); }
            if (st == 3 || st == 4) WAITV(15);
            else if (st == 5) { WAITV(15); op = poll_issue(p.ws, ofo); }
            else if (st == 6) WAITV(14);
            else if (st == 7) POLL_CHECK(p3i, 6, op, ofo);
            else if (st == 10 || st == 11) WAITV(12);
            else if (st == 9 || st == 12) WAITV(10); else if (st == 13) WAITV(6); else if (st == 14) WAITV(0);
            if (st < 15) RAWBAR();
        };
        pipe<72, 5>([&](auto ic) { constexpr int i = ic.v; lds_ptr r = ((i & 1) ? pb : pa).at(pslot(i >> 2) + ((i >> 1) & 1) * 2048); return trfrag(r, r + 1024); },
                    [&](auto ic, bf16x8 a) { constexpr int i = ic.v;
                        constexpr int ks = i >> 1, st = i >> 2, j = i & 3;
                        const bf16x8 b = __builtin_bit_cast(bf16x8, bf[ks % 28]);
                        if (ks < 28) af[i & 1] = MFMA(a, b, af[i & 1]); else as[i & 1] = MFMA(a, b, as[i & 1]);
                        if (st <= 12 && j < 2) issue_p(st + 5, j);
                        if constexpr (st < 6 && j >= 1) bf[7 + 3 * st + j] = ldws(rs, hb + (7 + 3 * st + j) * 1024);
                        if constexpr (st == 6 && (j & 1)) bf[14 + ks] = ldws(rs, hb + (14 + ks) * 1024);
                        if constexpr (st >= 8 && st <= 11 && (j & 1)) bf[ks - 16] = ldws(rs, ob + (ks - 16) * 1024);
                        if constexpr (st == 14 || st == 15) {
                            constexpr int nh = st - 14;
                            *(LAS f32x4*)(fd + nh * 128 + j * 32) = f32x4{af[nh][4 * j], af[nh][4 * j + 1], af[nh][4 * j + 2], af[nh][4 * j + 3]};
                        }
                        if (j == 3) phook(st);
                    });
        BAR();
        {
            lds_ptr d = sm + (kq < 3 ? simg(kq) + mh * 32 * 272 : mh ? 151552 : 0) + (lane & 31) * 272 + hi * 16;
#pragma unroll
            for (int nh = 0; nh < 2; ++nh)
#pragma unroll
                for (int c = 0; c < 4; ++c)
                    *(LAS f32x4*)(d + nh * 128 + c * 32) = f32x4{as[nh][4 * c], as[nh][4 * c + 1], as[nh][4 * c + 2], as[nh][4 * c + 3]};
        }
        lds_ptr d = sm + (tid >> 3) * 272 + (tid & 7) * 32;
        float gt[8];
        {
            f32x4 f0 = {}, f1 = {};
#pragma unroll
            for (int k = 0; k < 4; ++k) { f0 += *(LAS f32x4*)(d + fimg(k)); f1 += *(LAS f32x4*)(d + fimg(k) + 16); }
#pragma unroll
            for (int e = 0; e < 8; ++e) {
                const float f = e < 4 ? f0[e & 3] : f1[e & 3];
                gt[e] = f * __builtin_amdgcn_rcpf(1.0f + __builtin_amdgcn_exp2f(-1.4426950408889634f * f));
                asm volatile("" : "+v"(gt[e]));
            }
        }
        BAR();
        {
            f32x4 s0 = {}, s1 = {};
#pragma unroll
            for (int k = 0; k < 4; ++k) {
                const int so = k < 3 ? simg(k) : (w >= 4 ? 151552 - 32 * 272 : 0);
                s0 += *(LAS f32x4*)(d + so); s1 += *(LAS f32x4*)(d + so + 16);
            }
            float r[8];
#pragma unroll
            for (int e = 0; e < 8; ++e) {
                const float xv = (e & 1) ? bfhi(xres[e >> 1]) : bflo(xres[e >> 1]);
                r[e] = __builtin_fmaf(e < 4 ? s0[e & 3] : s1[e & 3], gt[e], xv);
            }
            u32x4 ov = {pk2(r[0], r[1]), pk2(r[2], r[3]), pk2(r[4], r[5]), pk2(r[6], r[7])};
            *(u32x4*)(p.out + (size_t)(rb * 64 + (tid >> 3)) * 896 + cb * 128 + (tid & 7) * 16) = ov;
            STAMP(26); STAMP_OUT;
        }
    }
}

extern "C" void kernel_launch(void* const* d_in, const int* in_sizes, int n_in, void* d_out, int out_size, void* d_ws, size_t ws_size,
                              hipStream_t stream) {
    (void)in_sizes; (void)n_in; (void)out_size; (void)ws_size;
    Params p;
    p.x = (const char*)d_in[0]; p.wqkv = (const char*)d_in[1]; p.wo = (const char*)d_in[2];
    p.wg = (const char*)d_in[4]; p.wu = (const char*)d_in[5]; p.wd = (const char*)d_in[6];
    p.out = (char*)d_out; p.ws = (char*)d_ws;
    (void)hipFuncSetAttribute((const void*)fused_kernel, hipFuncAttributeMaxDynamicSharedMemorySize, LDS_BYTES);
    fused_kernel<<<256, 512, LDS_BYTES, stream>>>(p);
}
```

```cpp
#include <hip/hip_runtime.h>
#include <hip/hip_bf16.h>

typedef __attribute__((ext_vector_type(8))) __bf16 bf16x8;
typedef __attribute__((ext_vector_type(2))) __bf16 bf16x2;
typedef __attribute__((ext_vector_type(16))) float f32x16;
typedef __attribute__((ext_vector_type(4))) float f32x4;
typedef __attribute__((ext_vector_type(2))) float f32x2;
typedef __attribute__((ext_vector_type(4))) unsigned u32x4;
typedef __attribute__((ext_vector_type(2))) unsigned u32x2;
typedef short v4i16_t __attribute__((ext_vector_type(4)));
typedef short v8i16_t __attribute__((ext_vector_type(8)));
typedef unsigned long long u64;
#define LAS __attribute__((address_space(3)))
typedef LAS char* lds_ptr;
typedef __attribute__((address_space(1))) u64 gu64;
#define DI __device__ __forceinline__
#define MFMA(a, b, c) __builtin_amdgcn_mfma_f32_32x32x16_bf16(a, b, c, 0, 0, 0)
#define RLX_AGENT __ATOMIC_RELAXED, __HIP_MEMORY_SCOPE_AGENT
#define WAITV(N) asm volatile("s_waitcnt vmcnt(" #N ")" ::: "memory")
#define BAR() asm volatile("s_waitcnt lgkmcnt(0)\n\ts_barrier" ::: "memory")
template <int N> DI void waitv() { asm volatile("s_waitcnt vmcnt(%0)" :: "i"(N) : "memory"); }

#define STAMPS 0

constexpr int AUX_SC1 = 16;
constexpr unsigned WS_EPOCH = 0;
constexpr unsigned WS_KVF = 4096;
constexpr unsigned WS_OF = WS_KVF + 65536;
constexpr unsigned WS_HF = WS_OF + 65536;
constexpr unsigned WS_CS = WS_HF + 131072;
constexpr unsigned WS_STAMP = 512 * 1024;
constexpr unsigned WS_KV = 1u << 20,
                    WS_O = 7u << 20, WS_H = 9u << 20, WS_END = 16u << 20;
constexpr int L_R = 0, L_S0 = 48640, L_GS = 28800, L_UP = 14336 + 128;
constexpr int L_Q = 32768, L_V = 0, L_K = 49152;
constexpr int L_CS_BIG = 43008, L_ST = 46592, L_RS = 41984;
constexpr int LDS_BYTES = 163840;
constexpr int CODE_BYTES = 40960;
DI int kvaddr(int fr) { return ((fr & 2) ? L_V : L_K) + (fr >> 2) * 2048 + (fr & 1) * 1024; }
DI constexpr int gslot(int i) { return L_S0 + i * L_GS; }
DI constexpr int pslot(int s) { return (s % 10 < 5 ? s % 10 + 5 : s % 10 < 7 ? s % 10 - 2 : s % 10 - 7) * 16384; }

struct Params {
    const char* x; const char* wqkv; const char* wo; const char* wg; const char* wu; const char* wd;
    char* out; char* ws;
};

DI void glds(const char* sbase, unsigned voff, unsigned lds) {
    asm volatile("s_mov_b32 m0, %2\n\ts_nop 0\n\tglobal_load_lds_dwordx4 %0, %1" :: "v"(voff), "s"(sbase), "s"(lds) : "memory");
}
DI void touch(const char* sbase, unsigned voff, unsigned lds) {
    unsigned keep;
    asm volatile("s_mov_b32 %0, m0\n\ts_mov_b32 m0, %3\n\ts_nop 0\n\tglobal_load_lds_dword %1, %2 sc1\n\ts_mov_b32 m0, %0"
                 : "=&s"(keep) : "v"(voff), "s"(sbase), "s"(lds) : "memory");
}
DI v4i16_t trr(lds_ptr p) { return __builtin_amdgcn_ds_read_tr16_b64_v4i16((LAS v4i16_t*)p); }
DI bf16x8 trfrag(lds_ptr p0, lds_ptr p1) {
    v4i16_t a = trr(p0), b = trr(p1);
    return __builtin_bit_cast(bf16x8, __builtin_shufflevector(a, b, 0, 1, 2, 3, 4, 5, 6, 7));
}
DI unsigned pk2(float a, float b) { f32x2 v = {a, b}; return __builtin_bit_cast(unsigned, __builtin_convertvector(v, bf16x2)); }
DI u32x4 pack8(const f32x16& c, int s) {
    u32x4 r = {pk2(c[8 * s], c[8 * s + 1]), pk2(c[8 * s + 2], c[8 * s + 3]), pk2(c[8 * s + 4], c[8 * s + 5]), pk2(c[8 * s + 6], c[8 * s + 7])};
    return r;
}
DI float bflo(unsigned u) { return __uint_as_float(u << 16); }
DI float bfhi(unsigned u) { return __uint_as_float(u & 0xffff0000u); }
DI float max3(float a, float b, float c) {
#pragma clang fp reassociate(on)
    return __builtin_amdgcn_fmed3f(__builtin_amdgcn_fmed3f(a, b, __builtin_inff()), c, __builtin_inff());
}
DI float xhalf(float v) {
    auto r = __builtin_amdgcn_permlane32_swap(__float_as_uint(v), __float_as_uint(v), false, false);
    return __uint_as_float((threadIdx.x & 32) ? r[0] : r[1]);
}
DI u32x4 ldws(__amdgpu_buffer_rsrc_t rs, unsigned off) { return __builtin_amdgcn_raw_buffer_load_b128(rs, (int)off, 0, AUX_SC1); }
DI void stws(__amdgpu_buffer_rsrc_t rs, unsigned off, u32x4 v) { __builtin_amdgcn_raw_buffer_store_b128(v, rs, (int)off, 0, AUX_SC1); }
DI void set_flag(char* ws, unsigned off, u64 tag, int lane) {
    (void)lane;
    __hip_atomic_store((gu64*)(ws + off), tag, RLX_AGENT);
}
DI void wait_flags(char* ws, unsigned off, u64 tag) {
    for (int spin = 0; spin < (1 << 20); ++spin) {
        u64 v = __hip_atomic_load((gu64*)(ws + off), RLX_AGENT);
        if (__all(v == tag)) break;
        __builtin_amdgcn_s_sleep(2);
    }
    __builtin_amdgcn_fence(__ATOMIC_ACQUIRE, "wavefront");
}
DI u64 poll_issue(char* ws, unsigned off) {
    u64 v;
    asm volatile("global_load_dwordx2 %0, %1, %2 sc1" : "=v"(v) : "v"(off), "s"(ws) : "memory");
    return v;
}
#define WAIT_SEL(c, NA, ...) asm volatile("s_cmp_lg_u32 %[sel], 0\n\ts_cbranch_scc1 1f\n\ts_waitcnt vmcnt(0)\n1:\n\ts_waitcnt vmcnt(" #NA ")" \
                                          : __VA_ARGS__ : [sel] "s"(c) : "scc", "memory")
#define POLL_CHECK(c, N, v, off) do { WAIT_SEL(c, N, "+v"(v)); if (!__all(v == tag)) wait_flags(p.ws, off, tag); } while (0)
DI u32x4 gran_issue(char* ws, unsigned off) {
    u32x4 v;
    asm volatile("global_load_dwordx4 %0, %1, %2 sc1" : "=v"(v) : "v"(off), "s"(ws) : "memory");
    return v;
}
DI bool gran_ok(u32x4 v, u64 tag) { return v[1] == (unsigned)tag && v[3] == (unsigned)(tag >> 32); }
DI unsigned gran_spin(__amdgpu_buffer_rsrc_t rs, unsigned off, u64 tag) {
    u32x4 v = ldws(rs, off);
    for (int spin = 0; spin < (1 << 20) && !__all(gran_ok(v, tag)); ++spin) { __builtin_amdgcn_s_sleep(2); v = ldws(rs, off); }
    return v[0];
}
#define STAMP(i)
#define STAMP_DECL
#define STAMP_OUT

#define SBAR() __builtin_amdgcn_sched_barrier(0)
#define RAWBAR() asm volatile("s_barrier" ::: "memory")
template <int V> struct IC { static constexpr int v = V; constexpr operator int() const { return V; } };
template <int I, int N, class F>
DI void sfor(F&& f) { if constexpr (I < N) { f(IC<I>{}); sfor<I + 1, N>(f); } }
template <int N, int D, class LD, class MM>
DI void pipe(LD ld, MM mm) {
    bf16x8 buf[D];
    sfor<0, D - 1>([&](auto i) { buf[i.v] = ld(i); });
    SBAR();
    sfor<0, N>([&](auto i) {
        if constexpr (i.v + D - 1 < N) buf[(i.v + D - 1) % D] = ld(IC<i.v + D - 1>{});
        SBAR();
        mm(i, buf[i.v % D]);
        SBAR();
    });
}
struct LB {
    unsigned b[3];
    DI LB(unsigned a) { b[0] = a; b[1] = a + 0xc000; b[2] = a + 0x18000 + 0x2000; asm volatile("" : "+v"(b[0]), "+v"(b[1]), "+v"(b[2])); }
    DI lds_ptr at(int off) const { return off < 0xc000 ? (lds_ptr)(size_t)b[0] + off : off < 0x1a000 ? (lds_ptr)(size_t)b[1] + (off - 0xc000) : (lds_ptr)(size_t)b[2] + (off - 0x1a000); }
};
DI float gelu_mul(float a, float b, float c1, float c2, float ir2) {
    float e = __builtin_amdgcn_exp2f(a * __builtin_fmaf(a * a, c2, c1));
    return a * b * __builtin_amdgcn_rcpf(__builtin_fmaf(e, ir2, ir2));
}

__global__ __launch_bounds__(512, 2) void fused_kernel(Params p) {
    extern __shared__ __attribute__((aligned(16))) char smem_[];
    lds_ptr sm = (lds_ptr)smem_;
    const unsigned smb = (unsigned)(size_t)sm;
    const int tid = threadIdx.x, lane = tid & 63;
    const int w = __builtin_amdgcn_readfirstlane(tid >> 6);
    const int hi = lane >> 5;
    const int xcd = blockIdx.x & 7, li = blockIdx.x >> 3;
#define TW 84
#define XMAP 1
    const int rt = ((xcd >> (3 - XMAP)) << (4 - XMAP)) + (li >> (1 + XMAP)), g = ((xcd & ((1 << (3 - XMAP)) - 1)) << (1 + XMAP)) + (li & ((2 << XMAP) - 1));
    const bool p3 = li < 28;
    const int rb = 4 * xcd + li / 7, cb = li % 7;
    const int rg = w & 3, cg = w >> 2;
    const int p3i = 28 - li > 0 ? 28 - li : 0;
    const __amdgpu_buffer_rsrc_t rs = __builtin_amdgcn_make_buffer_rsrc((void*)p.ws, 0, (int)WS_END, 0x00020000);
    STAMP_DECL; STAMP(0);

    const char* wq = p.wqkv + (56 * g - 8 * (g & 1));
    const char* wgb = p.wg + 224 * g;
    const char* wub = p.wu + 224 * g;
    u64 tag_ = poll_issue(p.ws, WS_EPOCH);
    {
        const char* xb = p.x + (size_t)(rt * 128 + w * 16) * 896;
        const unsigned xo = (lane >> 3) * 896 + ((lane & 7) ^ (lane >> 4)) * 16;
#pragma unroll
        for (int j = 0; j < 14; ++j)
            glds(xb + (j / 7) * 8 * 896 + (j % 7) * 128, j < 7 ? xo : xo ^ 64, smb + L_S0 + w * 14336 + j * 1024);
    }
    const unsigned qo = (lane >> 2) * 2688 + (lane & 3) * 16;
    auto issue_q = [&](int half, int pc) {
        if (w < 7) glds(wq + (pc >> 1) * 896 + (size_t)(half * 224 + (w * 2 + (pc & 1)) * 16) * 2688, qo,
                        smb + (half ? L_S0 : L_R) + (pc >> 1) * 14336 + (w * 2 + (pc & 1)) * 1024);
    };
#pragma unroll
    for (int j = 0; j < 6; ++j) issue_q(0, j);
    u32x4 cw[4];
    if (rt < 3 && w < 7) {
#pragma unroll
        for (int j = 0; j < 4; ++j)
            asm volatile("global_load_dwordx4 %0, %1, %2" : "=v"(cw[j]) : "v"(qo), "s"(wq + rt * 896 + (size_t)(w * 64 + j * 16) * 2688) : "memory");
    }
    if (w == 7) {
        const int c = xcd & 3, l0 = c + (c >> 1);
        int u = li * 64 + lane; u = u < 2016 ? u : 2015;
        touch(p.wqkv + 224 * 2688, (u / 9) * 2688 + ((u % 9) / 3) * 896 + (l0 + u % 3) * 128, smb + 163328);
        const int id = li < 14 ? li : 13, v = (id % 7) * 64 + lane;
        touch((id < 7 ? p.wg : p.wu) + c * 896, (v / 7) * 3584 + (v % 7) * 128, smb + 163328);
    }
    const int codew = li == 31 && w < CODE_BYTES / 8192;
    if (codew) {
        const char* pc;
        asm volatile("s_getpc_b64 %0" : "=s"(pc));
        touch(pc + w * 8192, lane * 128, smb + L_CS_BIG + w * 512);
    }
    unsigned goff[4];
    const int gp0 = cg ? 12 + rg * 4 : rg * 3;
#pragma unroll
    for (int j = 0; j < 4; ++j) {
        const int pc = gp0 + j, i = (pc % 14) * 64 + lane, row = i / 14, c = i - row * 14;
        goff[j] = row * 3584 + c * 16;
    }
    auto issue_g = [&](int st, int slot, int n) {
        const int pc = gp0 + n;
        if (n < 3 || cg) glds((pc >= 14 ? wub : wgb) + (size_t)st * 64 * 3584, goff[n], smb + slot + (pc >= 14 ? L_UP - 14336 : 0) + pc * 1024);
    };
    const unsigned poff = ((w & 1) * 16 + (lane >> 3)) * 896 + cb * 128 + (((lane & 7) ^ ((lane >> 2) & 4)) * 16);
    auto issue_p = [&](int st, int pc) {
        if (p3) {
            const int kq = w >> 1;
            const int hh = 4 * kq + st - 14;
            const char* b = st < 14 ? p.wd + (size_t)(448 * kq + 32 * st) * 896 : p.wo + (size_t)(28 * hh - 4 * (hh & 1)) * 896;
            glds(b + pc * 8 * 896, poff, smb + pslot(st) + w * 2048 + pc * 1024);
        }
    };
    u64 tag;
    auto cs_partial = [&]() {
        float cs[8];
#pragma unroll
        for (int e = 0; e < 4; ++e) {
            cs[2 * e] = (bflo(cw[0][e]) + bflo(cw[1][e])) + (bflo(cw[2][e]) + bflo(cw[3][e]));
            cs[2 * e + 1] = (bfhi(cw[0][e]) + bfhi(cw[1][e])) + (bfhi(cw[2][e]) + bfhi(cw[3][e]));
        }
#pragma unroll
        for (int e = 0; e < 8; ++e) {
            cs[e] += __builtin_bit_cast(float, __builtin_amdgcn_update_dpp(0, __builtin_bit_cast(int, cs[e]), 0x124, 0xf, 0xf, false));
            cs[e] += __builtin_bit_cast(float, __builtin_amdgcn_update_dpp(0, __builtin_bit_cast(int, cs[e]), 0x128, 0xf, 0xf, false));
        }
        {
            LAS f32x4* d = (LAS f32x4*)(sm + L_CS_BIG + w * 512 + (lane >> 4) * 128 + (lane & 3) * 32);
            d[0] = f32x4{cs[0], cs[1], cs[2], cs[3]}; d[1] = f32x4{cs[4], cs[5], cs[6], cs[7]};
        }
    };
    auto cs_publish = [&]() {
        float v = 0.f;
        LAS float* d = (LAS float*)(sm + L_CS_BIG + (lane & 31) * 4 + hi * 1792);
#pragma unroll
        for (int j = 0; j < 14; ++j) v += d[j * 32];
        v += xhalf(v);
        const unsigned h_ = pk2(v, v) & 0xffffu;
        const unsigned pv = h_ | (pk2(v - bflo(h_), 0.f) << 16);
        const int jb = rt - 3, tl = 3 + 2 * (jb & 3) + (((jb & 3) < 3) ? ((lane >> 4) & 1) : 0);
        const int slot = rt < 3 ? rt * 32 + (lane & 31) : tl * 32 + (jb >> 2) * 16 + (lane & 15);
        stws(rs, WS_CS + (g * 320 + slot) * 16, u32x4{pv, (unsigned)tag, pv, (unsigned)(tag >> 32)});
    };
    STAMP(1);
    {
        const int sel = w == 7 ? 0 : rt < 3 ? 10 : 6;
        asm volatile("s_cmp_eq_u32 %1, 10\n\ts_cbranch_scc1 1f\n\ts_cmp_eq_u32 %1, 6\n\ts_cbranch_scc1 2f\n\ts_waitcnt vmcnt(2)\n2:\n\ts_waitcnt vmcnt(6)\n1:\n\ts_waitcnt vmcnt(10)"
                     : "+v"(tag_) : "s"(sel) : "scc", "memory");
    }
    BAR();
    STAMP(2);
    tag = tag_ + 1;
    const int tile = rt * 4 + rg;
    const int q4 = (lane >> 2) & 3, p4 = lane & 3, g16 = (lane >> 4) & 1;
    const int kq = w >> 1, mh = w & 1, tt = p3 ? rb * 2 + mh : 0;
    const unsigned hfo = WS_HF + ((tt * 16 + 4 * kq) * 2 + (lane & 7)) * 64;
    const unsigned hb = WS_H + (tt * 112 + 28 * kq) * 1024 + lane * 16;
    u32x4 bf[28];
    auto phase1 = [&](auto cgc) {
    constexpr int cg = cgc.v;
    constexpr int NG = 3 + cg;
    auto issue_gc = [&](int st, int slot, int n) { if (n < NG) issue_g(st, slot, n); };
    u32x4 q1r[6];
    {
        const int wr = w < 7 ? w : 6;
#pragma unroll
        for (int j = 0; j < 6; ++j) q1r[j] = *(const u32x4*)(wq + (j >> 1) * 896 + (size_t)(224 + (wr * 2 + (j & 1)) * 16) * 2688 + qo);
    }
    bf16x8 xf[28];
    {
        const int t = lane & 31;
        lds_ptr xr = sm + L_S0 + (rg * 4 + (t >> 3)) * 7168 + (t & 7) * 128;
        const int sw = (t >> 1) & 7;
#pragma unroll
        for (int s_ = 0; s_ < 28; ++s_) { const int s = (s_ + 14 * cg) % 28, c = 2 * s + hi; xf[s] = *(LAS bf16x8*)(xr + (c >> 3) * 1024 + (((c ^ sw) & 7) * 16)); }
    }
    const bool csd = rt >= 3 && rt < 11, csw = csd && w < 7;
    {
        float s1a[4] = {0.f, 0.f, 0.f, 0.f}, s2a[4] = {0.f, 0.f, 0.f, 0.f};
        const bf16x2 ones = {(__bf16)1.0f, (__bf16)1.0f};
        auto stat = [&](const bf16x8& xv) {
            u32x4 u = __builtin_bit_cast(u32x4, xv);
#pragma unroll
            for (int e = 0; e < 4; ++e) {
                const unsigned ue = u[e];
                const bf16x2 v = __builtin_bit_cast(bf16x2, ue);
                s1a[e] = __builtin_amdgcn_fdot2_f32_bf16(v, ones, s1a[e], false);
                s2a[e] = __builtin_amdgcn_fdot2_f32_bf16(v, v, s2a[e], false);
            }
        };
        if (cg == 0) {
#pragma unroll
            for (int s = 0; s < 14; ++s) stat(xf[s]);
        } else {
#pragma unroll
            for (int s = 14; s < 28; ++s) stat(xf[s]);
        }
        f32x2 sv = {(s1a[0] + s1a[1]) + (s1a[2] + s1a[3]), (s2a[0] + s2a[1]) + (s2a[2] + s2a[3])};
        sv[0] += xhalf(sv[0]); sv[1] += xhalf(sv[1]);
        STAMP(3);
        WAITV(0);
        *(LAS f32x2*)(sm + L_ST + w * 256 + (lane & 31) * 8) = sv;
    }
    if (rt < 3 && w < 7) { asm volatile("" : "+v"(cw[0]), "+v"(cw[1]), "+v"(cw[2]), "+v"(cw[3])); cs_partial(); }
    BAR();
    if (w < 7) {
#pragma unroll
        for (int j = 0; j < 6; ++j) *(LAS u32x4*)(sm + L_S0 + (j >> 1) * 14336 + (w * 2 + (j & 1)) * 1024 + lane * 16) = q1r[j];
    }
    if (rt < 3 && w == 7) cs_publish();
    f32x16 acc[4];
    const float keeplo = ((g & 1) && hi == 0) ? 0.f : 1.f, keephi = (!(g & 1) && hi == 1) ? 0.f : 1.f;
    float rstd, ir2;
    bf16x8 mfr;
#pragma unroll
    for (int t = 0; t < 4; ++t) acc[t] = f32x16{};
    {
        STAMP(4);
        const unsigned cso = WS_CS + (g * 10 * 32 + (lane & 31)) * 16;
        u32x4 cg0v, cg1v;
        const LB qa(smb + (8 * hi + q4) * 64 + 32 * g16 + 8 * p4 + cg * 14336);
        auto qops = [&](int half, int n) {
            if (half == 0 && n >= 1 && n < 12 && n % 6 < 5 && n % 6 > 0) issue_gc(n / 6, gslot(2 + n / 6), n % 6 - 1);
            if (half == 1 && n == 2) { cg0v = gran_issue(p.ws, cso + cg * 512); cg1v = gran_issue(p.ws, cso + 1024); }
            if (half == 1 && n >= 4 && n < 12 && !(n & 1)) issue_gc(2, L_R, (n - 4) >> 1);
        };
        sfor<0, 2>([&](auto hc) {
            constexpr int half = hc.v, base = half ? L_S0 : L_R;
            pipe<28, 5>([&](auto ic) { constexpr int i = ic.v;
                            bf16x8 a;
                            if ((i & 1) == 0 || cg) { lds_ptr r = qa.at(base + (i >> 1) * 1024 + (i & 1) * 14336); a = trfrag(r, r + 256); } else asm("" : "=v"(a));
                            return a;
                        },
                        [&](auto ic, bf16x8 a) { constexpr int i = ic.v;
                            if (i & 1) { if (cg) acc[1] = MFMA(xf[half * 14 + (i >> 1)], a, acc[1]); }
                            else { acc[0] = MFMA(a, xf[half * 14 + (i >> 1)], acc[0]); qops(half, i >> 1); }
                        });
            if (half == 0) {
                const int id = li * 4 + rg;
                if (cg == 0 && XMAP == 1 && id < TW) {
                    const int u = (id % (TW / 2)) * 64 + lane;
                    touch((id < TW / 2 ? p.wg : p.wu) + (xcd & 3) * 896 + (size_t)(448 - TW * 32 / 7) * 3584, (u / 7) * 3584 + (u % 7) * 128, smb + 92160 + w * 256);
                }
                BAR();
                STAMP(5);
            }
        });
        STAMP(6);
        {
            const f32x2 sa = *(LAS f32x2*)(sm + L_ST + rg * 256 + (lane & 31) * 8), sb = *(LAS f32x2*)(sm + L_ST + (rg + 4) * 256 + (lane & 31) * 8);
            const float mean = (sa[0] + sb[0]) * (1.0f / 448.0f);
            ir2 = __builtin_fmaxf((sa[1] + sb[1]) * (1.0f / 448.0f) - mean * mean, 0.f) + 1e-5f;
            rstd = __builtin_amdgcn_rsqf(ir2);
            const float mh_ = -0.5f * mean;
            const unsigned m0_ = pk2(mh_, mh_), m1_ = pk2(mh_ - bflo(m0_ & 0xffffu), mh_ - bflo(m0_ & 0xffffu));
            mfr = __builtin_bit_cast(bf16x8, u32x4{m0_, m1_, 0u, 0u});
        }
        asm volatile("s_waitcnt vmcnt(%2)" : "+v"(cg0v), "+v"(cg1v) : "i"(NG) : "memory");
        RAWBAR();
        {
            unsigned c0 = cg0v[0], c1 = cg1v[0];
            if (!__all(gran_ok(cg0v, tag) && gran_ok(cg1v, tag))) { c0 = gran_spin(rs, cso + cg * 512, tag); c1 = gran_spin(rs, cso + 1024, tag); }
            acc[0] = MFMA(__builtin_bit_cast(bf16x8, u32x4{c0, c0, 0u, 0u}), mfr, acc[0]);
            if (cg) acc[1] = MFMA(mfr, __builtin_bit_cast(bf16x8, u32x4{c1, c1, 0u, 0u}), acc[1]);
        }
        const unsigned fo = WS_KV + (tile * 16 + g) * 4096 + lane * 16;
        if (cg == 0) {
            const float qs = 0.1889822365046136f * 1.4426950408889634f * rstd;
            const float qlo = qs * keeplo, qhi = qs * keephi;
#pragma unroll
            for (int r = 0; r < 16; ++r) acc[0][r] *= r < 4 ? qlo : r >= 12 ? qhi : qs;
            LAS u32x4* qd = (LAS u32x4*)(sm + L_Q + rg * 2048 + lane * 16);
            qd[0] = pack8(acc[0], 0); qd[64] = pack8(acc[0], 1);
        } else {
            const float vpad = ((g & 1) ? ((lane & 31) < 4) : ((lane & 31) >= 28)) ? 1.f : 0.f, vkeep = 1.f - vpad;
            LAS float* rt_ = (LAS float*)(sm + L_RS + rg * 128);
            rt_[lane & 31] = rstd;
#pragma unroll
            for (int c = 0; c < 4; ++c) {
                const f32x4 rr = *(LAS f32x4*)(rt_ + 8 * c + 4 * hi);
#pragma unroll
                for (int e = 0; e < 4; ++e) { acc[1][4 * c + e] = __builtin_fmaf(acc[1][4 * c + e] * vkeep, rr[e], vpad); acc[0][4 * c + e] *= rstd; }
            }
            stws(rs, fo, pack8(acc[0], 0)); stws(rs, fo + 1024, pack8(acc[0], 1));
            stws(rs, fo + 2048, pack8(acc[1], 0)); stws(rs, fo + 3072, pack8(acc[1], 1));
        }
        acc[0] = f32x16{}; acc[1] = f32x16{};
        STAMP(7);

        STAMP(8);
    }
    u32x4 kvr[6], csg[4];
    const unsigned csgo = WS_CS + ((g * 10 + 3 + 4 * cg) * 32 + (lane & 31)) * 16;
    const char* csb = (rt >= 7 ? wub : wgb) + ((rt - 3) & 3) * 64 + (((rt - 3) & 3) < 3 ? (lane & 3) : (lane & 1)) * 16 + (size_t)(w * 64 + (lane >> 2)) * 3584;
    u64 kvp;
    unsigned kvfo;
    { int kt = 4 * rt - 8 + ((6 * w) >> 2) + (lane & 1); kt = kt < 0 ? 0 : kt; kvfo = WS_KVF + (kt * 16 + g) * 64; }
    f32x16 o;
    float m;
    LAS f32x4* scr = (LAS f32x4*)(sm + (rg < 2 ? 24576 : 65536) + rg * 4096 + lane * 16);
    const bool padl = (g & 1) ? (hi == 0) : (hi == 1);
    {
        const LB ga(smb + (8 * hi + q4) * 224 + L_UP * g16 + 8 * p4 + cg * 128);
        auto ghook = [&](int st) {
            if (st == 0) STAMP(9); if (st == 1) STAMP(10); if (st == 4) STAMP(11); if (st == 6) STAMP(12);
            if (st == 0) {
                waitv<2 * NG + 4 * cg>();
            } else if (st == 1) { if (csw) waitv<NG + 3>(); else waitv<NG + 1>(); }
            else if (st == 2) {
                if (csw) waitv<2 * NG + 4>(); else waitv<2 * NG>();
                kvp = poll_issue(p.ws, kvfo);
            } else if (st == 3) {
                asm volatile("s_waitcnt vmcnt(0)" : "+v"(kvp) :: "memory");
                if (!__all(kvp == tag)) wait_flags(p.ws, kvfo, tag);
                if (csw) { asm volatile("" : "+v"(cw[0]), "+v"(cw[1]), "+v"(cw[2]), "+v"(cw[3])); cs_partial(); }
                BAR();
                if (csd && w == 7) cs_publish();
                return;
            }
            else if (st == 5) asm volatile("s_waitcnt lgkmcnt(0)" ::: "memory");
            RAWBAR();
        };
        auto gdma = [&](int st, int n) {
            if (st == 1 && n == 2 && cg) {
                waitv<10>();
                set_flag(p.ws, WS_KVF + (tile * 16 + g) * 64, tag, lane);
            }
            if (st == 0) { issue_gc(3, gslot(0), n); issue_gc(4, gslot(1), n); }
            if (st == 1 || st == 2) issue_gc(4 + st, gslot(1 + st), n);
            if ((st == 1 || st == 2) && n < 2 && csw)
                asm volatile("global_load_dwordx4 %0, %1, off" : "=v"(cw[2 * (st & 2) / 2 + n]) : "v"(csb + (size_t)(2 * (st & 2) / 2 + n) * 16 * 3584) : "memory");
            if (st == 4 && n < 3) {
#pragma unroll
                for (int e = 0; e < 2; ++e) {
                    const int fr = 6 * w + 2 * n + e;
                    int kt = 4 * rt - 8 + (fr >> 2); kt = kt < 0 ? 0 : kt;
                    kvr[2 * n + e] = ldws(rs, WS_KV + (kt * 16 + g) * 4096 + (fr & 3) * 1024 + lane * 16);
                }
            }
            if (st == 5 && n == 1) {
#pragma unroll
                for (int e = 0; e < 6; ++e) *(LAS u32x4*)(sm + kvaddr(6 * w + e) + lane * 16) = kvr[e];
            }
            if (st == 5 && n >= 2) issue_p(0, n - 2);
            if (st == 6 && n == 0) {
#pragma unroll
                for (int t = 0; t < 4 - cg; ++t) csg[t] = gran_issue(p.ws, csgo + t * 512);
            }
            if (st == 6 && (n == 1 || n == 2)) { issue_p(n, 0); issue_p(n, 1); }
        };
        f32x16 sc[5];
        bf16x8 qf0, qf1, fa, fb;
        auto side = [&](auto kc) {
            constexpr int k = kc.v;
            auto kv = [&](int j, int part) { return *(LAS bf16x8*)(sm + kvaddr(4 * (rg + 5 * cg + (j < 4 ? j : 4 - cg)) + part) + lane * 16); };
            if constexpr (k == 0) {
                LAS bf16x8* ql_ = (LAS bf16x8*)(sm + L_Q + rg * 2048 + lane * 16);
                qf0 = ql_[0]; qf1 = ql_[64]; fa = kv(0, 0); fb = kv(0, 1); m = -3.0e38f;
            } else if constexpr (k <= 5) {
                constexpr int j = k - 1;
                const bf16x8 ka = fa, kb = fb;
                if constexpr (j < 4) { fa = kv(j + 1, 0); fb = kv(j + 1, 1); } else fa = kv(0, 2);
                f32x16 z = f32x16{};
                if constexpr (j == 0 || j == 3) {
                    const int ql = lane & 31;
                    const float big = ((j == 0) == (cg == 0)) ? 0x1p100f : 0.f;
                    const float b0 = (float)(j ? ql - 4 * hi : 4 * hi - ql - 1) * big;
#pragma unroll
                    for (int r = 0; r < 16; ++r) z[r] = __builtin_fminf(__builtin_fmaf(j ? -big : big, (float)((r & 3) + 8 * (r >> 2)), b0), 0.f);
                }
                if (j < 4 || cg == 0) { z = MFMA(ka, qf0, z); sc[j] = MFMA(kb, qf1, z); }
            } else if constexpr (k <= 10) {
                constexpr int j = k - 6;
                if (j < 4 || cg == 0) {
                    if (tile - (cg ? 3 : 8) + j < 0) {
                        asm volatile("");
#pragma unroll
                        for (int r = 0; r < 16; ++r) sc[j][r] = -1.0e30f;
                    }
#pragma unroll
                    for (int r = 0; r < 16; r += 2) m = max3(m, sc[j][r], sc[j][r + 1]);
                }
                if constexpr (j == 4) m = __builtin_fmaxf(m, xhalf(m));
            } else if constexpr (k <= 20) {
                constexpr int e = k - 11, j = e >> 1, h = e & 1;
                const bf16x8 va = fa;
                if constexpr (e < 9) fa = kv((e + 1) >> 1, 2 + ((e + 1) & 1));
                if (j < 4 || cg == 0) {
#pragma unroll
                    for (int r = 0; r < 8; ++r) sc[j][8 * h + r] = __builtin_amdgcn_exp2f(sc[j][8 * h + r] - m);
                    o = MFMA(va, __builtin_bit_cast(bf16x8, pack8(sc[j], h)), e == 0 ? f32x16{} : o);
                }
            } else if constexpr (k == 22) {
                if (cg == 0) {
                    if (g & 1) o[1] = padl ? m : o[1]; else o[13] = padl ? m : o[13];
#pragma unroll
                    for (int c = 0; c < 4; ++c) scr[c * 64] = f32x4{o[4 * c], o[4 * c + 1], o[4 * c + 2], o[4 * c + 3]};
                }
            }
        };
        auto gaddr = [&](int ks) {
            const int st = ks >> 2;
            const int slot = st == 0 ? gslot(2) : st == 1 ? gslot(3) : st == 2 ? L_R : st == 3 ? gslot(0) : st == 4 ? gslot(1) : st == 5 ? gslot(2) : gslot(3);
            return ga.at(slot + (ks & 3) * 3584);
        };
        pipe<112, 5>([&](auto ic) { constexpr int i = ic.v;
                         bf16x8 a;
                         if ((i & 3) < 3 || cg == 0) { lds_ptr r = gaddr(i >> 2) + 32 * (i & 3); a = trfrag(r, r + 896); } else asm("" : "=v"(a));
                         return a;
                     },
                     [&](auto ic, bf16x8 a) { constexpr int i = ic.v;
                         if ((i & 3) < 3 || cg == 0) acc[i & 3] = MFMA(a, xf[i >> 2], acc[i & 3]);
                         if ((i & 3) == 2 * cg) gdma(i >> 4, (i >> 2) & 3);
                         if ((i & 15) == 15) ghook(i >> 4);
                     });
    __builtin_amdgcn_s_setprio(1);
    if constexpr (cg == 0) WAIT_SEL(p3i, 4, "+v"(csg[0]), "+v"(csg[1]), "+v"(csg[2]), "+v"(csg[3]));
    else WAIT_SEL(p3i, 4, "+v"(csg[0]), "+v"(csg[1]), "+v"(csg[2]));
    {
        unsigned csv[4];
        bool ok = true;
#pragma unroll
        for (int t = 0; t < 4 - cg; ++t) { csv[t] = csg[t][0]; ok = ok && gran_ok(csg[t], tag); }
        if (!__all(ok)) {
#pragma unroll
            for (int t = 0; t < 4 - cg; ++t) csv[t] = gran_spin(rs, csgo + t * 512, tag);
        }
#pragma unroll
        for (int t = 0; t < 4 - cg; ++t) acc[t] = MFMA(__builtin_bit_cast(bf16x8, u32x4{csv[t], csv[t], 0u, 0u}), mfr, acc[t]);
    }
    STAMP(13);
    {
        const float gc1 = -2.0f * 0.7978845608028654f * 1.4426950408889634f * rstd, gc2 = gc1 * 0.044715f * rstd * rstd;
#pragma unroll
        for (int t = 0; t < 4; ++t) {
            if (t < 3 || cg == 0) {
                u32x4 hv;
#pragma unroll
                for (int e = 0; e < 4; ++e)
                    hv[e] = pk2(gelu_mul(acc[t][2 * e], acc[t][8 + 2 * e], gc1, gc2, ir2), gelu_mul(acc[t][2 * e + 1], acc[t][9 + 2 * e], gc1, gc2, ir2));
                stws(rs, WS_H + (tile * 112 + 7 * g + 4 * cg + t) * 1024 + lane * 16, hv);
                SBAR();
            }
        }
    }
    __builtin_amdgcn_s_setprio(0);
    STAMP(14);
    u64 hp;
    sfor<0, 23>([&](auto kc) {
        side(kc);
        if (kc.v == 6) {
            WAITV(0);
            set_flag(p.ws, WS_HF + ((tile * 16 + g) * 2 + cg) * 64, tag, lane);
            STAMP(15);
        }
        if (kc.v == 6) {
            const int id = li * 8 + w;
            if (id < 245) touch(id < 196 ? p.wd + id * 8192 : p.wo + (id - 196) * 8192, lane * 128, smb + 40960);
        }
        if (kc.v >= 7 && kc.v < 11) issue_p(3 + ((kc.v - 7) >> 1), (kc.v - 7) & 1);
        if (kc.v == 19 && p3) hp = poll_issue(p.ws, hfo);
        if (kc.v >= 6) SBAR();
    });
    STAMP(16);
    if (p3) {
        asm volatile("s_waitcnt vmcnt(0)" : "+v"(hp) :: "memory");
        if (!__all(hp == tag)) wait_flags(p.ws, hfo, tag);
        STAMP(17);
#pragma unroll
        for (int i = 0; i < 8; ++i) bf[i] = ldws(rs, hb + i * 1024);
    }
    BAR();
    STAMP(18);
    if (cg == 1) {
        f32x16 oa;
#pragma unroll
        for (int c = 0; c < 4; ++c) { f32x4 v = scr[c * 64]; oa[4 * c] = v[0]; oa[4 * c + 1] = v[1]; oa[4 * c + 2] = v[2]; oa[4 * c + 3] = v[3]; }
        float ma = (g & 1) ? oa[1] : oa[13], la = (g & 1) ? oa[0] : oa[12], lb = (g & 1) ? o[0] : o[12];
        float ma2 = xhalf(ma), la2 = xhalf(la), lb2 = xhalf(lb);
        ma = padl ? ma : ma2; la = padl ? la : la2; lb = padl ? lb : lb2;
        const float mm = __builtin_fmaxf(ma, m);
        float wa = __builtin_amdgcn_exp2f(ma - mm), wb = __builtin_amdgcn_exp2f(m - mm);
        const float inv = __builtin_amdgcn_rcpf(la * wa + lb * wb);
        wa *= inv; wb *= inv;
#pragma unroll
        for (int r = 0; r < 16; ++r) {
            o[r] = oa[r] * wa + o[r] * wb;
            if (r < 4) o[r] *= keeplo;
            if (r >= 12) o[r] *= keephi;
        }
        const unsigned fo = WS_O + (tile * 16 + g) * 2048 + lane * 16;
        stws(rs, fo, pack8(o, 0)); stws(rs, fo + 1024, pack8(o, 1));
        if (!p3) { WAITV(0); set_flag(p.ws, WS_OF + (tile * 16 + g) * 64, tag, lane); }
    }
    }
    };
    if (cg == 0) phase1(IC<0>{}); else phase1(IC<1>{});
    STAMP(19);
    if (!p3) {
        if (blockIdx.x == 255 && w == 0) {
#pragma unroll
            for (int j = 0; j < 4; ++j) { int id = j * 64 + lane; wait_flags(p.ws, WS_KVF + ((id >> 4) * 64 + (id & 15)) * 64, tag); }
            set_flag(p.ws, WS_EPOCH, tag, lane);
        }
        WAITV(0);
        STAMP_OUT;
        return;
    }
    {
        const unsigned ofo = WS_OF + (tt * 16 + 4 * kq + (lane & 3)) * 64;
        const unsigned ob = WS_O + (tt * 16 + 4 * kq) * 2048 + lane * 16;
        const u32x4 xres = *(const u32x4*)(p.x + (size_t)(rb * 64 + (tid >> 3)) * 896 + cb * 128 + (tid & 7) * 16);
        u64 op;
        f32x16 af[2], as[2];
        af[0] = f32x16{}; af[1] = f32x16{}; as[0] = f32x16{}; as[1] = f32x16{};
        const int lo = kq * 4096 + (4 * hi + q4) * 128 + 32 * g16 + 8 * p4;
        const int n0 = ((q4 >> 1) & 1) * 64;
        const LB pa(smb + lo + n0), pb(smb + lo + (64 - n0));
        auto fimg = [](int k) { return k == 0 ? 16384 : 64512 + k * 17408; };
        auto simg = [](int k) { return k == 0 ? 33792 : k == 1 ? 51200 : k == 2 ? 134144 : 0; };
        lds_ptr fd = sm + fimg(kq) + (mh * 32 + (lane & 31)) * 272 + hi * 16;
        auto phook = [&](int st) {
            if (st == 0) STAMP(21); if (st == 6) STAMP(22); if (st == 8) STAMP(23); if (st == 13) STAMP(24); if (st == 17) STAMP(25);
            if (st == 2 && cg) { WAITV(15); set_flag(p.ws, WS_OF + (tile * 16 + g) * 64, tag, lane); }
            if (st == 3 || st == 4) WAITV(15);
            else if (st == 5) { WAITV(15); op = poll_issue(p.ws, ofo); }
            else if (st == 6) WAITV(14);
            else if (st == 7) POLL_CHECK(p3i, 6, op, ofo);
            else if (st == 10 || st == 11) WAITV(12);
            else if (st == 9 || st == 12) WAITV(10); else if (st == 13) WAITV(6); else if (st == 14) WAITV(0);
            if (st < 15) RAWBAR();
        };
        pipe<72, 5>([&](auto ic) { constexpr int i = ic.v; lds_ptr r = ((i & 1) ? pb : pa).at(pslot(i >> 2) + ((i >> 1) & 1) * 2048); return trfrag(r, r + 1024); },
                    [&](auto ic, bf16x8 a) { constexpr int i = ic.v;
                        constexpr int ks = i >> 1, st = i >> 2, j = i & 3;
                        const bf16x8 b = __builtin_bit_cast(bf16x8, bf[ks % 28]);
                        if (ks < 28) af[i & 1] = MFMA(a, b, af[i & 1]); else as[i & 1] = MFMA(a, b, as[i & 1]);
                        if (st <= 12 && j < 2) issue_p(st + 5, j);
                        if constexpr (st < 6 && j >= 1) bf[7 + 3 * st + j] = ldws(rs, hb + (7 + 3 * st + j) * 1024);
                        if constexpr (st == 6 && (j & 1)) bf[14 + ks] = ldws(rs, hb + (14 + ks) * 1024);
                        if constexpr (st >= 8 && st <= 11 && (j & 1)) bf[ks - 16] = ldws(rs, ob + (ks - 16) * 1024);
                        if constexpr (st == 14 || st == 15) {
                            constexpr int nh = st - 14;
                            *(LAS f32x4*)(fd + nh * 128 + j * 32) = f32x4{af[nh][4 * j], af[nh][4 * j + 1], af[nh][4 * j + 2], af[nh][4 * j + 3]};
                        }
                        if (j == 3) phook(st);
                    });
        BAR();
        {
            lds_ptr d = sm + (kq < 3 ? simg(kq) + mh * 32 * 272 : mh ? 151552 : 0) + (lane & 31) * 272 + hi * 16;
#pragma unroll
            for (int nh = 0; nh < 2; ++nh)
#pragma unroll
                for (int c = 0; c < 4; ++c)
                    *(LAS f32x4*)(d + nh * 128 + c * 32) = f32x4{as[nh][4 * c], as[nh][4 * c + 1], as[nh][4 * c + 2], as[nh][4 * c + 3]};
        }
        lds_ptr d = sm + (tid >> 3) * 272 + (tid & 7) * 32;
        float gt[8];
        {
            f32x4 f0 = {}, f1 = {};
#pragma unroll
            for (int k = 0; k < 4; ++k) { f0 += *(LAS f32x4*)(d + fimg(k)); f1 += *(LAS f32x4*)(d + fimg(k) + 16); }
#pragma unroll
            for (int e = 0; e < 8; ++e) {
                const float f = e < 4 ? f0[e & 3] : f1[e & 3];
                gt[e] = f * __builtin_amdgcn_rcpf(1.0f + __builtin_amdgcn_exp2f(-1.4426950408889634f * f));
                asm volatile("" : "+v"(gt[e]));
            }
        }
        BAR();
        {
            f32x4 s0 = {}, s1 = {};
#pragma unroll
            for (int k = 0; k < 4; ++k) {
                const int so = k < 3 ? simg(k) : (w >= 4 ? 151552 - 32 * 272 : 0);
                s0 += *(LAS f32x4*)(d + so); s1 += *(LAS f32x4*)(d + so + 16);
            }
            float r[8];
#pragma unroll
            for (int e = 0; e < 8; ++e) {
                const float xv = (e & 1) ? bfhi(xres[e >> 1]) : bflo(xres[e >> 1]);
                r[e] = __builtin_fmaf(e < 4 ? s0[e & 3] : s1[e & 3], gt[e], xv);
            }
            u32x4 ov = {pk2(r[0], r[1]), pk2(r[2], r[3]), pk2(r[4], r[5]), pk2(r[6], r[7])};
            *(u32x4*)(p.out + (size_t)(rb * 64 + (tid >> 3)) * 896 + cb * 128 + (tid & 7) * 16) = ov;
            STAMP(26); STAMP_OUT;
        }
    }
}

extern "C" void kernel_launch(void* const* d_in, const int* in_sizes, int n_in, void* d_out, int out_size, void* d_ws, size_t ws_size,
                              hipStream_t stream) {
    (void)in_sizes; (void)n_in; (void)out_size; (void)ws_size;
    Params p;
    p.x = (const char*)d_in[0]; p.wqkv = (const char*)d_in[1]; p.wo = (const char*)d_in[2];
    p.wg = (const char*)d_in[4]; p.wu = (const char*)d_in[5]; p.wd = (const char*)d_in[6];
    p.out = (char*)d_out; p.ws = (char*)d_ws;
    (void)hipFuncSetAttribute((const void*)fused_kernel, hipFuncAttributeMaxDynamicSharedMemorySize, LDS_BYTES);
    fused_kernel<<<256, 512, LDS_BYTES, stream>>>(p);
}
```

```cpp
#include <hip/hip_runtime.h>
#include <hip/hip_bf16.h>

typedef __attribute__((ext_vector_type(8))) __bf16 bf16x8;
typedef __attribute__((ext_vector_type(2))) __bf16 bf16x2;
typedef __attribute__((ext_vector_type(16))) float f32x16;
typedef __attribute__((ext_vector_type(4))) float f32x4;
typedef __attribute__((ext_vector_type(2))) float f32x2;
typedef __attribute__((ext_vector_type(4))) unsigned u32x4;
typedef __attribute__((ext_vector_type(2))) unsigned u32x2;
typedef short v4i16_t __attribute__((ext_vector_type(4)));
typedef short v8i16_t __attribute__((ext_vector_type(8)));
typedef unsigned long long u64;
#define LAS __attribute__((address_space(3)))
typedef LAS char* lds_ptr;
typedef __attribute__((address_space(1))) u64 gu64;
#define DI __device__ __forceinline__
#define MFMA(a, b, c) __builtin_amdgcn_mfma_f32_32x32x16_bf16(a, b, c, 0, 0, 0)
#define RLX_AGENT __ATOMIC_RELAXED, __HIP_MEMORY_SCOPE_AGENT
#define WAITV(N) asm volatile("s_waitcnt vmcnt(" #N ")" ::: "memory")
#define BAR() asm volatile("s_waitcnt lgkmcnt(0)\n\ts_barrier" ::: "memory")
template <int N> DI void waitv() { asm volatile("s_waitcnt vmcnt(%0)" :: "i"(N) : "memory"); }

#define STAMPS 0

constexpr int AUX_SC1 = 16;
constexpr unsigned WS_EPOCH = 0;
constexpr unsigned WS_KVF = 4096;
constexpr unsigned WS_OF = WS_KVF + 65536;
constexpr unsigned WS_HF = WS_OF + 65536;
constexpr unsigned WS_CS = WS_HF + 131072;
constexpr unsigned WS_STAMP = 512 * 1024;
constexpr unsigned WS_KV = 1u << 20,
                    WS_O = 7u << 20, WS_H = 9u << 20, WS_END = 16u << 20;
constexpr int L_R = 0, L_S0 = 48640, L_GS = 28800, L_UP = 14336 + 128;
constexpr int L_Q = 32768, L_V = 0, L_K = 49152;
constexpr int L_CS_BIG = 43008, L_ST = 46592, L_RS = 41984;
constexpr int LDS_BYTES = 163840;
constexpr int CODE_BYTES = 40960;
DI int kvaddr(int fr) { return ((fr & 2) ? L_V : L_K) + (fr >> 2) * 2048 + (fr & 1) * 1024; }
DI constexpr int gslot(int i) { return L_S0 + i * L_GS; }
DI constexpr int pslot(int s) { return (s % 10 < 5 ? s % 10 + 5 : s % 10 < 7 ? s % 10 - 2 : s % 10 - 7) * 16384; }

struct Params {
    const char* x; const char* wqkv; const char* wo; const char* wg; const char* wu; const char* wd;
    char* out; char* ws;
};

DI void glds(const char* sbase, unsigned voff, unsigned lds) {
    asm volatile("s_mov_b32 m0, %2\n\ts_nop 0\n\tglobal_load_lds_dwordx4 %0, %1" :: "v"(voff), "s"(sbase), "s"(lds) : "memory");
}
DI void touch(const char* sbase, unsigned voff, unsigned lds) {
    unsigned keep;
    asm volatile("s_mov_b32 %0, m0\n\ts_mov_b32 m0, %3\n\ts_nop 0\n\tglobal_load_lds_dword %1, %2 sc1\n\ts_mov_b32 m0, %0"
                 : "=&s"(keep) : "v"(voff), "s"(sbase), "s"(lds) : "memory");
}
DI v4i16_t trr(lds_ptr p) { return __builtin_amdgcn_ds_read_tr16_b64_v4i16((LAS v4i16_t*)p); }
DI bf16x8 trfrag(lds_ptr p0, lds_ptr p1) {
    v4i16_t a = trr(p0), b = trr(p1);
    return __builtin_bit_cast(bf16x8, __builtin_shufflevector(a, b, 0, 1, 2, 3, 4, 5, 6, 7));
}
DI unsigned pk2(float a, float b) { f32x2 v = {a, b}; return __builtin_bit_cast(unsigned, __builtin_convertvector(v, bf16x2)); }
DI u32x4 pack8(const f32x16& c, int s) {
    u32x4 r = {pk2(c[8 * s], c[8 * s + 1]), pk2(c[8 * s + 2], c[8 * s + 3]), pk2(c[8 * s + 4], c[8 * s + 5]), pk2(c[8 * s + 6], c[8 * s + 7])};
    return r;
}
DI float bflo(unsigned u) { return __uint_as_float(u << 16); }
DI float bfhi(unsigned u) { return __uint_as_float(u & 0xffff0000u); }
DI float rbf(float a) { return bflo(pk2(a, a)); }
DI float max3(float a, float b, float c) {
#pragma clang fp reassociate(on)
    return __builtin_amdgcn_fmed3f(__builtin_amdgcn_fmed3f(a, b, __builtin_inff()), c, __builtin_inff());
}
DI float xhalf(float v) {
    auto r = __builtin_amdgcn_permlane32_swap(__float_as_uint(v), __float_as_uint(v), false, false);
    return __uint_as_float((threadIdx.x & 32) ? r[0] : r[1]);
}
DI u32x4 ldws(__amdgpu_buffer_rsrc_t rs, unsigned off) { return __builtin_amdgcn_raw_buffer_load_b128(rs, (int)off, 0, AUX_SC1); }
DI void stws(__amdgpu_buffer_rsrc_t rs, unsigned off, u32x4 v) { __builtin_amdgcn_raw_buffer_store_b128(v, rs, (int)off, 0, AUX_SC1); }
DI void set_flag(char* ws, unsigned off, u64 tag, int lane) {
    (void)lane;
    __hip_atomic_store((gu64*)(ws + off), tag, RLX_AGENT);
}
DI void wait_flags(char* ws, unsigned off, u64 tag) {
    for (int spin = 0; spin < (1 << 20); ++spin) {
        u64 v = __hip_atomic_load((gu64*)(ws + off), RLX_AGENT);
        if (__all(v == tag)) break;
        __builtin_amdgcn_s_sleep(2);
    }
    __builtin_amdgcn_fence(__ATOMIC_ACQUIRE, "wavefront");
}
DI u64 poll_issue(char* ws, unsigned off) {
    u64 v;
    asm volatile("global_load_dwordx2 %0, %1, %2 sc1" : "=v"(v) : "v"(off), "s"(ws) : "memory");
    return v;
}
#define WAIT_SEL(c, NA, ...) asm volatile("s_cmp_lg_u32 %[sel], 0\n\ts_cbranch_scc1 1f\n\ts_waitcnt vmcnt(0)\n1:\n\ts_waitcnt vmcnt(" #NA ")" \
                                          : __VA_ARGS__ : [sel] "s"(c) : "scc", "memory")
#define POLL_CHECK(c, N, v, off) do { WAIT_SEL(c, N, "+v"(v)); if (!__all(v == tag)) wait_flags(p.ws, off, tag); } while (0)
DI u32x4 gran_issue(char* ws, unsigned off) {
    u32x4 v;
    asm volatile("global_load_dwordx4 %0, %1, %2 sc1" : "=v"(v) : "v"(off), "s"(ws) : "memory");
    return v;
}
DI bool gran_ok(u32x4 v, u64 tag) { return v[1] == (unsigned)tag && v[3] == (unsigned)(tag >> 32); }
DI unsigned gran_spin(__amdgpu_buffer_rsrc_t rs, unsigned off, u64 tag) {
    u32x4 v = ldws(rs, off);
    for (int spin = 0; spin < (1 << 20) && !__all(gran_ok(v, tag)); ++spin) { __builtin_amdgcn_s_sleep(2); v = ldws(rs, off); }
    return v[0];
}
#define STAMP(i)
#define STAMP_DECL
#define STAMP_OUT

#define SBAR() __builtin_amdgcn_sched_barrier(0)
#define RAWBAR() asm volatile("s_barrier" ::: "memory")
template <int V> struct IC { static constexpr int v = V; constexpr operator int() const { return V; } };
template <int I, int N, class F>
DI void sfor(F&& f) { if constexpr (I < N) { f(IC<I>{}); sfor<I + 1, N>(f); } }
template <int N, int D, class LD, class MM>
DI void pipe(LD ld, MM mm) {
    bf16x8 buf[D];
    sfor<0, D - 1>([&](auto i) { buf[i.v] = ld(i); });
    SBAR();
    sfor<0, N>([&](auto i) {
        if constexpr (i.v + D - 1 < N) buf[(i.v + D - 1) % D] = ld(IC<i.v + D - 1>{});
        SBAR();
        mm(i, buf[i.v % D]);
        SBAR();
    });
}
struct LB {
    unsigned b[3];
    DI LB(unsigned a) { b[0] = a; b[1] = a + 0xc000; b[2] = a + 0x18000 + 0x2000; asm volatile("" : "+v"(b[0]), "+v"(b[1]), "+v"(b[2])); }
    DI lds_ptr at(int off) const { return off < 0xc000 ? (lds_ptr)(size_t)b[0] + off : off < 0x1a000 ? (lds_ptr)(size_t)b[1] + (off - 0xc000) : (lds_ptr)(size_t)b[2] + (off - 0x1a000); }
};
DI float gelu_mul(float a, float b, float c1, float c2, float ir2) {
    float e = __builtin_amdgcn_exp2f(a * __builtin_fmaf(a * a, c2, c1));
    return a * b * __builtin_amdgcn_rcpf(__builtin_fmaf(e, ir2, ir2));
}

__global__ __launch_bounds__(512, 2) void fused_kernel(Params p) {
    extern __shared__ __attribute__((aligned(16))) char smem_[];
    lds_ptr sm = (lds_ptr)smem_;
    const unsigned smb = (unsigned)(size_t)sm;
    const int tid = threadIdx.x, lane = tid & 63;
    const int w = __builtin_amdgcn_readfirstlane(tid >> 6);
    const int hi = lane >> 5;
    const int xcd = blockIdx.x & 7, li = blockIdx.x >> 3;
#define TW 84
#define XMAP 1
    const int rt = ((xcd >> (3 - XMAP)) << (4 - XMAP)) + (li >> (1 + XMAP)), g = ((xcd & ((1 << (3 - XMAP)) - 1)) << (1 + XMAP)) + (li & ((2 << XMAP) - 1));
    const bool p3 = li < 28;
    const int rb = 4 * xcd + li / 7, cb = li % 7;
    const int rg = w & 3, cg = w >> 2;
    const int p3i = 28 - li > 0 ? 28 - li : 0;
    const __amdgpu_buffer_rsrc_t rs = __builtin_amdgcn_make_buffer_rsrc((void*)p.ws, 0, (int)WS_END, 0x00020000);
    STAMP_DECL; STAMP(0);

    const char* wq = p.wqkv + (56 * g - 8 * (g & 1));
    const char* wgb = p.wg + 224 * g;
    const char* wub = p.wu + 224 * g;
    u64 tag_ = poll_issue(p.ws, WS_EPOCH);
    {
        const char* xb = p.x + (size_t)(rt * 128 + w * 16) * 896;
        const unsigned xo = (lane >> 3) * 896 + ((lane & 7) ^ (lane >> 4)) * 16;
#pragma unroll
        for (int j = 0; j < 14; ++j)
            glds(xb + (j / 7) * 8 * 896 + (j % 7) * 128, j < 7 ? xo : xo ^ 64, smb + L_S0 + w * 14336 + j * 1024);
    }
    const unsigned qo = (lane >> 2) * 2688 + (lane & 3) * 16;
    auto issue_q = [&](int half, int pc) {
        if (w < 7) glds(wq + (pc >> 1) * 896 + (size_t)(half * 224 + (w * 2 + (pc & 1)) * 16) * 2688, qo,
                        smb + (half ? L_S0 : L_R) + (pc >> 1) * 14336 + (w * 2 + (pc & 1)) * 1024);
    };
#pragma unroll
    for (int j = 0; j < 6; ++j) issue_q(0, j);
    u32x4 cw[4];
    if (rt < 3 && w < 7) {
#pragma unroll
        for (int j = 0; j < 4; ++j)
            asm volatile("global_load_dwordx4 %0, %1, %2" : "=v"(cw[j]) : "v"(qo), "s"(wq + rt * 896 + (size_t)(w * 64 + j * 16) * 2688) : "memory");
    }
    if (w == 7) {
        const int c = xcd & 3, l0 = c + (c >> 1);
        int u = li * 64 + lane; u = u < 2016 ? u : 2015;
        touch(p.wqkv + 224 * 2688, (u / 9) * 2688 + ((u % 9) / 3) * 896 + (l0 + u % 3) * 128, smb + 163328);
        const int id = li < 14 ? li : 13, v = (id % 7) * 64 + lane;
        touch((id < 7 ? p.wg : p.wu) + c * 896, (v / 7) * 3584 + (v % 7) * 128, smb + 163328);
    }
    const int codew = li == 31 && w < CODE_BYTES / 8192;
    if (codew) {
        const char* pc;
        asm volatile("s_getpc_b64 %0" : "=s"(pc));
        touch(pc + w * 8192, lane * 128, smb + L_CS_BIG + w * 512);
    }
    unsigned goff[4];
    const int gp0 = cg ? 12 + rg * 4 : rg * 3;
#pragma unroll
    for (int j = 0; j < 4; ++j) {
        const int pc = gp0 + j, i = (pc % 14) * 64 + lane, row = i / 14, c = i - row * 14;
        goff[j] = row * 3584 + c * 16;
    }
    auto issue_g = [&](int st, int slot, int n) {
        const int pc = gp0 + n;
        if (n < 3 || cg) glds((pc >= 14 ? wub : wgb) + (size_t)st * 64 * 3584, goff[n], smb + slot + (pc >= 14 ? L_UP - 14336 : 0) + pc * 1024);
    };
    const unsigned poff = ((w & 1) * 16 + (lane >> 3)) * 896 + cb * 128 + (((lane & 7) ^ ((lane >> 2) & 4)) * 16);
    auto issue_p = [&](int st, int pc) {
        if (p3) {
            const int kq = w >> 1;
            const int hh = 4 * kq + st - 14;
            const char* b = st < 14 ? p.wd + (size_t)(448 * kq + 32 * st) * 896 : p.wo + (size_t)(28 * hh - 4 * (hh & 1)) * 896;
            glds(b + pc * 8 * 896, poff, smb + pslot(st) + w * 2048 + pc * 1024);
        }
    };
    u64 tag;
    auto cs_partial = [&]() {
        float cs[8];
#pragma unroll
        for (int e = 0; e < 4; ++e) {
            cs[2 * e] = (bflo(cw[0][e]) + bflo(cw[1][e])) + (bflo(cw[2][e]) + bflo(cw[3][e]));
            cs[2 * e + 1] = (bfhi(cw[0][e]) + bfhi(cw[1][e])) + (bfhi(cw[2][e]) + bfhi(cw[3][e]));
        }
#pragma unroll
        for (int e = 0; e < 8; ++e) {
            cs[e] += __builtin_bit_cast(float, __builtin_amdgcn_update_dpp(0, __builtin_bit_cast(int, cs[e]), 0x124, 0xf, 0xf, false));
            cs[e] += __builtin_bit_cast(float, __builtin_amdgcn_update_dpp(0, __builtin_bit_cast(int, cs[e]), 0x128, 0xf, 0xf, false));
        }
        {
            LAS f32x4* d = (LAS f32x4*)(sm + L_CS_BIG + w * 512 + (lane >> 4) * 128 + (lane & 3) * 32);
            d[0] = f32x4{cs[0], cs[1], cs[2], cs[3]}; d[1] = f32x4{cs[4], cs[5], cs[6], cs[7]};
        }
    };
    auto cs_publish = [&]() {
        float v = 0.f;
        LAS float* d = (LAS float*)(sm + L_CS_BIG + (lane & 31) * 4 + hi * 1792);
#pragma unroll
        for (int j = 0; j < 14; ++j) v += d[j * 32];
        v += xhalf(v);
        const unsigned h_ = pk2(v, v) & 0xffffu;
        const unsigned pv = h_ | (pk2(v - bflo(h_), 0.f) << 16);
        const int jb = rt - 3, tl = 3 + 2 * (jb & 3) + (((jb & 3) < 3) ? ((lane >> 4) & 1) : 0);
        const int slot = rt < 3 ? rt * 32 + (lane & 31) : tl * 32 + (jb >> 2) * 16 + (lane & 15);
        stws(rs, WS_CS + (g * 320 + slot) * 16, u32x4{pv, (unsigned)tag, pv, (unsigned)(tag >> 32)});
    };
    STAMP(1);
    {
        const int sel = w == 7 ? 0 : rt < 3 ? 10 : 6;
        asm volatile("s_cmp_eq_u32 %1, 10\n\ts_cbranch_scc1 1f\n\ts_cmp_eq_u32 %1, 6\n\ts_cbranch_scc1 2f\n\ts_waitcnt vmcnt(2)\n2:\n\ts_waitcnt vmcnt(6)\n1:\n\ts_waitcnt vmcnt(10)"
                     : "+v"(tag_) : "s"(sel) : "scc", "memory");
    }
    BAR();
    STAMP(2);
    tag = tag_ + 1;
    const int tile = rt * 4 + rg;
    const int q4 = (lane >> 2) & 3, p4 = lane & 3, g16 = (lane >> 4) & 1;
    const int kq = w >> 1, mh = w & 1, tt = p3 ? rb * 2 + mh : 0;
    const unsigned hfo = WS_HF + ((tt * 16 + 4 * kq) * 2 + (lane & 7)) * 64;
    const unsigned hb = WS_H + (tt * 112 + 28 * kq) * 1024 + lane * 16;
    u32x4 bf[28];
    auto phase1 = [&](auto cgc) {
    constexpr int cg = cgc.v;
    constexpr int NG = 3 + cg;
    auto issue_gc = [&](int st, int slot, int n) { if (n < NG) issue_g(st, slot, n); };
    u32x4 q1r[6];
    {
        const int wr = w < 7 ? w : 6;
#pragma unroll
        for (int j = 0; j < 6; ++j) q1r[j] = *(const u32x4*)(wq + (j >> 1) * 896 + (size_t)(224 + (wr * 2 + (j & 1)) * 16) * 2688 + qo);
    }
    bf16x8 xf[28];
    {
        const int t = lane & 31;
        lds_ptr xr = sm + L_S0 + (rg * 4 + (t >> 3)) * 7168 + (t & 7) * 128;
        const int sw = (t >> 1) & 7;
#pragma unroll
        for (int s_ = 0; s_ < 28; ++s_) { const int s = (s_ + 14 * cg) % 28, c = 2 * s + hi; xf[s] = *(LAS bf16x8*)(xr + (c >> 3) * 1024 + (((c ^ sw) & 7) * 16)); }
    }
    const bool csd = rt >= 3 && rt < 11, csw = csd && w < 7;
    {
        float s1a[4] = {0.f, 0.f, 0.f, 0.f}, s2a[4] = {0.f, 0.f, 0.f, 0.f};
        const bf16x2 ones = {(__bf16)1.0f, (__bf16)1.0f};
        auto stat = [&](const bf16x8& xv) {
            u32x4 u = __builtin_bit_cast(u32x4, xv);
#pragma unroll
            for (int e = 0; e < 4; ++e) {
                const unsigned ue = u[e];
                const bf16x2 v = __builtin_bit_cast(bf16x2, ue);
                s1a[e] = __builtin_amdgcn_fdot2_f32_bf16(v, ones, s1a[e], false);
                s2a[e] = __builtin_amdgcn_fdot2_f32_bf16(v, v, s2a[e], false);
            }
        };
        if (cg == 0) {
#pragma unroll
            for (int s = 0; s < 14; ++s) stat(xf[s]);
        } else {
#pragma unroll
            for (int s = 14; s < 28; ++s) stat(xf[s]);
        }
        f32x2 sv = {(s1a[0] + s1a[1]) + (s1a[2] + s1a[3]), (s2a[0] + s2a[1]) + (s2a[2] + s2a[3])};
        sv[0] += xhalf(sv[0]); sv[1] += xhalf(sv[1]);
        STAMP(3);
        WAITV(0);
        *(LAS f32x2*)(sm + L_ST + w * 256 + (lane & 31) * 8) = sv;
    }
    if (rt < 3 && w < 7) { asm volatile("" : "+v"(cw[0]), "+v"(cw[1]), "+v"(cw[2]), "+v"(cw[3])); cs_partial(); }
    BAR();
    if (w < 7) {
#pragma unroll
        for (int j = 0; j < 6; ++j) *(LAS u32x4*)(sm + L_S0 + (j >> 1) * 14336 + (w * 2 + (j & 1)) * 1024 + lane * 16) = q1r[j];
    }
    if (rt < 3 && w == 7) cs_publish();
    f32x16 acc[4];
    const float keeplo = ((g & 1) && hi == 0) ? 0.f : 1.f, keephi = (!(g & 1) && hi == 1) ? 0.f : 1.f;
    float rstd, ir2;
    bf16x8 mfr;
#pragma unroll
    for (int t = 0; t < 4; ++t) acc[t] = f32x16{};
    {
        STAMP(4);
        const unsigned cso = WS_CS + (g * 10 * 32 + (lane & 31)) * 16;
        u32x4 cg0v, cg1v;
        const LB qa(smb + (8 * hi + q4) * 64 + 32 * g16 + 8 * p4 + cg * 14336);
        auto qops = [&](int half, int n) {
            if (half == 0 && n >= 1 && n < 12 && n % 6 < 5 && n % 6 > 0) issue_gc(n / 6, gslot(2 + n / 6), n % 6 - 1);
            if (half == 1 && n == 2) { cg0v = gran_issue(p.ws, cso + cg * 512); cg1v = gran_issue(p.ws, cso + 1024); }
            if (half == 1 && n >= 4 && n < 12 && !(n & 1)) issue_gc(2, L_R, (n - 4) >> 1);
        };
        sfor<0, 2>([&](auto hc) {
            constexpr int half = hc.v, base = half ? L_S0 : L_R;
            pipe<28, 5>([&](auto ic) { constexpr int i = ic.v;
                            bf16x8 a;
                            if ((i & 1) == 0 || cg) { lds_ptr r = qa.at(base + (i >> 1) * 1024 + (i & 1) * 14336); a = trfrag(r, r + 256); } else asm("" : "=v"(a));
                            return a;
                        },
                        [&](auto ic, bf16x8 a) { constexpr int i = ic.v;
                            if (i & 1) { if (cg) acc[1] = MFMA(xf[half * 14 + (i >> 1)], a, acc[1]); }
                            else { acc[0] = MFMA(a, xf[half * 14 + (i >> 1)], acc[0]); qops(half, i >> 1); }
                        });
            if (half == 0) {
                const int id = li * 4 + rg;
                if (cg == 0 && XMAP == 1 && id < TW) {
                    const int u = (id % (TW / 2)) * 64 + lane;
                    touch((id < TW / 2 ? p.wg : p.wu) + (xcd & 3) * 896 + (size_t)(448 - TW * 32 / 7) * 3584, (u / 7) * 3584 + (u % 7) * 128, smb + 92160 + w * 256);
                }
                BAR();
                STAMP(5);
            }
        });
        STAMP(6);
        {
            const f32x2 sa = *(LAS f32x2*)(sm + L_ST + rg * 256 + (lane & 31) * 8), sb = *(LAS f32x2*)(sm + L_ST + (rg + 4) * 256 + (lane & 31) * 8);
            const float mean = (sa[0] + sb[0]) * (1.0f / 448.0f);
            ir2 = __builtin_fmaxf((sa[1] + sb[1]) * (1.0f / 448.0f) - mean * mean, 0.f) + 1e-5f;
            rstd = __builtin_amdgcn_rsqf(ir2);
            const float mh_ = -0.5f * mean;
            const unsigned m0_ = pk2(mh_, mh_), m1_ = pk2(mh_ - bflo(m0_ & 0xffffu), mh_ - bflo(m0_ & 0xffffu));
            mfr = __builtin_bit_cast(bf16x8, u32x4{m0_, m1_, 0u, 0u});
        }
        asm volatile("s_waitcnt vmcnt(%2)" : "+v"(cg0v), "+v"(cg1v) : "i"(NG) : "memory");
        RAWBAR();
        {
            unsigned c0 = cg0v[0], c1 = cg1v[0];
            if (!__all(gran_ok(cg0v, tag) && gran_ok(cg1v, tag))) { c0 = gran_spin(rs, cso + cg * 512, tag); c1 = gran_spin(rs, cso + 1024, tag); }
            acc[0] = MFMA(__builtin_bit_cast(bf16x8, u32x4{c0, c0, 0u, 0u}), mfr, acc[0]);
            if (cg) acc[1] = MFMA(mfr, __builtin_bit_cast(bf16x8, u32x4{c1, c1, 0u, 0u}), acc[1]);
        }
        const unsigned fo = WS_KV + (tile * 16 + g) * 4096 + lane * 16;
        if (cg == 0) {
            const float qs = 0.1889822365046136f * 1.4426950408889634f * rstd;
            const float qlo = qs * keeplo, qhi = qs * keephi;
#pragma unroll
            for (int r = 0; r < 16; ++r) acc[0][r] *= r < 4 ? qlo : r >= 12 ? qhi : qs;
            LAS u32x4* qd = (LAS u32x4*)(sm + L_Q + rg * 2048 + lane * 16);
            qd[0] = pack8(acc[0], 0); qd[64] = pack8(acc[0], 1);
        } else {
            const float vpad = ((g & 1) ? ((lane & 31) < 4) : ((lane & 31) >= 28)) ? 1.f : 0.f, vkeep = 1.f - vpad;
            LAS float* rt_ = (LAS float*)(sm + L_RS + rg * 128);
            rt_[lane & 31] = rstd;
#pragma unroll
            for (int c = 0; c < 4; ++c) {
                const f32x4 rr = *(LAS f32x4*)(rt_ + 8 * c + 4 * hi);
#pragma unroll
                for (int e = 0; e < 4; ++e) { acc[1][4 * c + e] = __builtin_fmaf(acc[1][4 * c + e] * vkeep, rr[e], vpad); acc[0][4 * c + e] *= rstd; }
            }
            stws(rs, fo, pack8(acc[0], 0)); stws(rs, fo + 1024, pack8(acc[0], 1));
            stws(rs, fo + 2048, pack8(acc[1], 0)); stws(rs, fo + 3072, pack8(acc[1], 1));
        }
        acc[0] = f32x16{}; acc[1] = f32x16{};
        STAMP(7);

        STAMP(8);
    }
    u32x4 kvr[6], csg[4];
    const unsigned csgo = WS_CS + ((g * 10 + 3 + 4 * cg) * 32 + (lane & 31)) * 16;
    const char* csb = (rt >= 7 ? wub : wgb) + ((rt - 3) & 3) * 64 + (((rt - 3) & 3) < 3 ? (lane & 3) : (lane & 1)) * 16 + (size_t)(w * 64 + (lane >> 2)) * 3584;
    u64 kvp;
    unsigned kvfo;
    { int kt = 4 * rt - 8 + ((6 * w) >> 2) + (lane & 1); kt = kt < 0 ? 0 : kt; kvfo = WS_KVF + (kt * 16 + g) * 64; }
    f32x16 o;
    float m;
    LAS f32x4* scr = (LAS f32x4*)(sm + (rg < 2 ? 24576 : 65536) + rg * 4096 + lane * 16);
    const bool padl = (g & 1) ? (hi == 0) : (hi == 1);
    {
        const LB ga(smb + (8 * hi + q4) * 224 + L_UP * g16 + 8 * p4 + cg * 128);
        auto ghook = [&](int st) {
            if (st == 0) STAMP(9); if (st == 1) STAMP(10); if (st == 4) STAMP(11); if (st == 6) STAMP(12);
            if (st == 0) {
                waitv<2 * NG + 4 * cg>();
            } else if (st == 1) { if (csw) waitv<NG + 3>(); else waitv<NG + 1>(); }
            else if (st == 2) {
                if (csw) waitv<2 * NG + 4>(); else waitv<2 * NG>();
                kvp = poll_issue(p.ws, kvfo);
            } else if (st == 3) {
                asm volatile("s_waitcnt vmcnt(0)" : "+v"(kvp) :: "memory");
                if (!__all(kvp == tag)) wait_flags(p.ws, kvfo, tag);
                if (csw) { asm volatile("" : "+v"(cw[0]), "+v"(cw[1]), "+v"(cw[2]), "+v"(cw[3])); cs_partial(); }
                BAR();
                if (csd && w == 7) cs_publish();
                return;
            }
            else if (st == 5) asm volatile("s_waitcnt lgkmcnt(0)" ::: "memory");
            RAWBAR();
        };
        auto gdma = [&](int st, int n) {
            if (st == 1 && n == 2 && cg) {
                waitv<10>();
                set_flag(p.ws, WS_KVF + (tile * 16 + g) * 64, tag, lane);
            }
            if (st == 0) { issue_gc(3, gslot(0), n); issue_gc(4, gslot(1), n); }
            if (st == 1 || st == 2) issue_gc(4 + st, gslot(1 + st), n);
            if ((st == 1 || st == 2) && n < 2 && csw)
                asm volatile("global_load_dwordx4 %0, %1, off" : "=v"(cw[2 * (st & 2) / 2 + n]) : "v"(csb + (size_t)(2 * (st & 2) / 2 + n) * 16 * 3584) : "memory");
            if (st == 4 && n < 3) {
#pragma unroll
                for (int e = 0; e < 2; ++e) {
                    const int fr = 6 * w + 2 * n + e;
                    int kt = 4 * rt - 8 + (fr >> 2); kt = kt < 0 ? 0 : kt;
                    kvr[2 * n + e] = ldws(rs, WS_KV + (kt * 16 + g) * 4096 + (fr & 3) * 1024 + lane * 16);
                }
            }
            if (st == 5 && n == 1) {
#pragma unroll
                for (int e = 0; e < 6; ++e) *(LAS u32x4*)(sm + kvaddr(6 * w + e) + lane * 16) = kvr[e];
            }
            if (st == 5 && n >= 2) issue_p(0, n - 2);
            if (st == 6 && n == 0) {
#pragma unroll
                for (int t = 0; t < 4 - cg; ++t) csg[t] = gran_issue(p.ws, csgo + t * 512);
            }
            if (st == 6 && (n == 1 || n == 2)) { issue_p(n, 0); issue_p(n, 1); }
        };
        f32x16 sc[5];
        bf16x8 qf0, qf1, fa, fb;
        auto side = [&](auto kc) {
            constexpr int k = kc.v;
            auto kv = [&](int j, int part) { return *(LAS bf16x8*)(sm + kvaddr(4 * (rg + 5 * cg + (j < 4 ? j : 4 - cg)) + part) + lane * 16); };
            if constexpr (k == 0) {
                LAS bf16x8* ql_ = (LAS bf16x8*)(sm + L_Q + rg * 2048 + lane * 16);
                qf0 = ql_[0]; qf1 = ql_[64]; fa = kv(0, 0); fb = kv(0, 1); m = -3.0e38f;
            } else if constexpr (k <= 5) {
                constexpr int j = k - 1;
                const bf16x8 ka = fa, kb = fb;
                if constexpr (j < 4) { fa = kv(j + 1, 0); fb = kv(j + 1, 1); } else fa = kv(0, 2);
                f32x16 z = f32x16{};
                if constexpr (j == 0 || j == 3) {
                    const int ql = lane & 31;
                    const float big = ((j == 0) == (cg == 0)) ? 0x1p100f : 0.f;
                    const float b0 = (float)(j ? ql - 4 * hi : 4 * hi - ql - 1) * big;
#pragma unroll
                    for (int r = 0; r < 16; ++r) z[r] = __builtin_fminf(__builtin_fmaf(j ? -big : big, (float)((r & 3) + 8 * (r >> 2)), b0), 0.f);
                }
                if (j < 4 || cg == 0) { z = MFMA(ka, qf0, z); sc[j] = MFMA(kb, qf1, z); }
            } else if constexpr (k <= 10) {
                constexpr int j = k - 6;
                if (j < 4 || cg == 0) {
                    if (tile - (cg ? 3 : 8) + j < 0) {
                        asm volatile("");
#pragma unroll
                        for (int r = 0; r < 16; ++r) sc[j][r] = -1.0e30f;
                    }
#pragma unroll
                    for (int r = 0; r < 16; r += 2) m = max3(m, sc[j][r], sc[j][r + 1]);
                }
                if constexpr (j == 4) m = __builtin_fmaxf(m, xhalf(m));
            } else if constexpr (k <= 20) {
                constexpr int e = k - 11, j = e >> 1, h = e & 1;
                const bf16x8 va = fa;
                if constexpr (e < 9) fa = kv((e + 1) >> 1, 2 + ((e + 1) & 1));
                if (j < 4 || cg == 0) {
#pragma unroll
                    for (int r = 0; r < 8; ++r) sc[j][8 * h + r] = __builtin_amdgcn_exp2f(sc[j][8 * h + r] - m);
                    o = MFMA(va, __builtin_bit_cast(bf16x8, pack8(sc[j], h)), e == 0 ? f32x16{} : o);
                }
            } else if constexpr (k == 22) {
                if (cg == 0) {
                    if (g & 1) o[1] = padl ? m : o[1]; else o[13] = padl ? m : o[13];
#pragma unroll
                    for (int c = 0; c < 4; ++c) scr[c * 64] = f32x4{o[4 * c], o[4 * c + 1], o[4 * c + 2], o[4 * c + 3]};
                }
            }
        };
        auto gaddr = [&](int ks) {
            const int st = ks >> 2;
            const int slot = st == 0 ? gslot(2) : st == 1 ? gslot(3) : st == 2 ? L_R : st == 3 ? gslot(0) : st == 4 ? gslot(1) : st == 5 ? gslot(2) : gslot(3);
            return ga.at(slot + (ks & 3) * 3584);
        };
        pipe<112, 5>([&](auto ic) { constexpr int i = ic.v;
                         bf16x8 a;
                         if ((i & 3) < 3 || cg == 0) { lds_ptr r = gaddr(i >> 2) + 32 * (i & 3); a = trfrag(r, r + 896); } else asm("" : "=v"(a));
                         return a;
                     },
                     [&](auto ic, bf16x8 a) { constexpr int i = ic.v;
                         if ((i & 3) < 3 || cg == 0) acc[i & 3] = MFMA(a, xf[i >> 2], acc[i & 3]);
                         if ((i & 3) == 2 * cg) gdma(i >> 4, (i >> 2) & 3);
                         if ((i & 15) == 15) ghook(i >> 4);
                     });
    __builtin_amdgcn_s_setprio(1);
    if constexpr (cg == 0) WAIT_SEL(p3i, 4, "+v"(csg[0]), "+v"(csg[1]), "+v"(csg[2]), "+v"(csg[3]));
    else WAIT_SEL(p3i, 4, "+v"(csg[0]), "+v"(csg[1]), "+v"(csg[2]));
    {
        unsigned csv[4];
        bool ok = true;
#pragma unroll
        for (int t = 0; t < 4 - cg; ++t) { csv[t] = csg[t][0]; ok = ok && gran_ok(csg[t], tag); }
        if (!__all(ok)) {
#pragma unroll
            for (int t = 0; t < 4 - cg; ++t) csv[t] = gran_spin(rs, csgo + t * 512, tag);
        }
#pragma unroll
        for (int t = 0; t < 4 - cg; ++t) acc[t] = MFMA(__builtin_bit_cast(bf16x8, u32x4{csv[t], csv[t], 0u, 0u}), mfr, acc[t]);
    }
    STAMP(13);
    {
        const float gc1 = -2.0f * 0.7978845608028654f * 1.4426950408889634f * rstd, gc2 = gc1 * 0.044715f * rstd * rstd;
#pragma unroll
        for (int t = 0; t < 4; ++t) {
            if (t < 3 || cg == 0) {
                u32x4 hv;
#pragma unroll
                for (int e = 0; e < 4; ++e)
                    hv[e] = pk2(gelu_mul(acc[t][2 * e], acc[t][8 + 2 * e], gc1, gc2, ir2), gelu_mul(acc[t][2 * e + 1], acc[t][9 + 2 * e], gc1, gc2, ir2));
                stws(rs, WS_H + (tile * 112 + 7 * g + 4 * cg + t) * 1024 + lane * 16, hv);
                SBAR();
            }
        }
    }
    __builtin_amdgcn_s_setprio(0);
    STAMP(14);
    u64 hp;
    sfor<0, 23>([&](auto kc) {
        side(kc);
        if (kc.v == 6) {
            WAITV(0);
            set_flag(p.ws, WS_HF + ((tile * 16 + g) * 2 + cg) * 64, tag, lane);
            STAMP(15);
        }
        if (kc.v == 6) {
            const int id = li * 8 + w;
            if (id < 245) touch(id < 196 ? p.wd + id * 8192 : p.wo + (id - 196) * 8192, lane * 128, smb + 40960);
        }
        if (kc.v >= 7 && kc.v < 11) issue_p(3 + ((kc.v - 7) >> 1), (kc.v - 7) & 1);
        if (kc.v == 19 && p3) hp = poll_issue(p.ws, hfo);
        if (kc.v >= 6) SBAR();
    });
    STAMP(16);
    if (p3) {
        asm volatile("s_waitcnt vmcnt(0)" : "+v"(hp) :: "memory");
        if (!__all(hp == tag)) wait_flags(p.ws, hfo, tag);
        STAMP(17);
#pragma unroll
        for (int i = 0; i < 8; ++i) bf[i] = ldws(rs, hb + i * 1024);
    }
    BAR();
    STAMP(18);
    if (cg == 1) {
        f32x16 oa;
#pragma unroll
        for (int c = 0; c < 4; ++c) { f32x4 v = scr[c * 64]; oa[4 * c] = v[0]; oa[4 * c + 1] = v[1]; oa[4 * c + 2] = v[2]; oa[4 * c + 3] = v[3]; }
        float ma = (g & 1) ? oa[1] : oa[13], la = (g & 1) ? oa[0] : oa[12], lb = (g & 1) ? o[0] : o[12];
        float ma2 = xhalf(ma), la2 = xhalf(la), lb2 = xhalf(lb);
        ma = padl ? ma : ma2; la = padl ? la : la2; lb = padl ? lb : lb2;
        const float mm = __builtin_fmaxf(ma, m);
        float wa = __builtin_amdgcn_exp2f(ma - mm), wb = __builtin_amdgcn_exp2f(m - mm);
        const float inv = __builtin_amdgcn_rcpf(la * wa + lb * wb);
        wa *= inv; wb *= inv;
#pragma unroll
        for (int r = 0; r < 16; ++r) {
            o[r] = oa[r] * wa + o[r] * wb;
            if (r < 4) o[r] *= keeplo;
            if (r >= 12) o[r] *= keephi;
        }
        const unsigned fo = WS_O + (tile * 16 + g) * 2048 + lane * 16;
        stws(rs, fo, pack8(o, 0)); stws(rs, fo + 1024, pack8(o, 1));
        if (!p3) { WAITV(0); set_flag(p.ws, WS_OF + (tile * 16 + g) * 64, tag, lane); }
    }
    }
    };
    if (cg == 0) phase1(IC<0>{}); else phase1(IC<1>{});
    STAMP(19);
    if (!p3) {
        if (blockIdx.x == 255 && w == 0) {
#pragma unroll
            for (int j = 0; j < 4; ++j) { int id = j * 64 + lane; wait_flags(p.ws, WS_KVF + ((id >> 4) * 64 + (id & 15)) * 64, tag); }
            set_flag(p.ws, WS_EPOCH, tag, lane);
        }
        WAITV(0);
        STAMP_OUT;
        return;
    }
    {
        const unsigned ofo = WS_OF + (tt * 16 + 4 * kq + (lane & 3)) * 64;
        const unsigned ob = WS_O + (tt * 16 + 4 * kq) * 2048 + lane * 16;
        const u32x4 xres = *(const u32x4*)(p.x + (size_t)(rb * 64 + (tid >> 3)) * 896 + cb * 128 + (tid & 7) * 16);
        u64 op;
        f32x16 af[2], as[2];
        af[0] = f32x16{}; af[1] = f32x16{}; as[0] = f32x16{}; as[1] = f32x16{};
        const int lo = kq * 4096 + (4 * hi + q4) * 128 + 32 * g16 + 8 * p4;
        const int n0 = ((q4 >> 1) & 1) * 64;
        const LB pa(smb + lo + n0), pb(smb + lo + (64 - n0));
        auto fimg = [](int k) { return k == 0 ? 16384 : 64512 + k * 17408; };
        auto simg = [](int k) { return k == 0 ? 33792 : k == 1 ? 51200 : k == 2 ? 134144 : 0; };
        lds_ptr fd = sm + fimg(kq) + (mh * 32 + (lane & 31)) * 272 + hi * 16;
        auto phook = [&](int st) {
            if (st == 0) STAMP(21); if (st == 6) STAMP(22); if (st == 8) STAMP(23); if (st == 13) STAMP(24); if (st == 17) STAMP(25);
            if (st == 2 && cg) { WAITV(15); set_flag(p.ws, WS_OF + (tile * 16 + g) * 64, tag, lane); }
            if (st == 3 || st == 4) WAITV(15);
            else if (st == 5) { WAITV(15); op = poll_issue(p.ws, ofo); }
            else if (st == 6) WAITV(14);
            else if (st == 7) POLL_CHECK(p3i, 6, op, ofo);
            else if (st == 10 || st == 11) WAITV(12);
            else if (st == 9 || st == 12) WAITV(10); else if (st == 13) WAITV(6); else if (st == 14) WAITV(0);
            if (st < 15) RAWBAR();
        };
        pipe<72, 5>([&](auto ic) { constexpr int i = ic.v; lds_ptr r = ((i & 1) ? pb : pa).at(pslot(i >> 2) + ((i >> 1) & 1) * 2048); return trfrag(r, r + 1024); },
                    [&](auto ic, bf16x8 a) { constexpr int i = ic.v;
                        constexpr int ks = i >> 1, st = i >> 2, j = i & 3;
                        const bf16x8 b = __builtin_bit_cast(bf16x8, bf[ks % 28]);
                        if (ks < 28) af[i & 1] = MFMA(a, b, af[i & 1]); else as[i & 1] = MFMA(a, b, as[i & 1]);
                        if (st <= 12 && j < 2) issue_p(st + 5, j);
                        if constexpr (st < 6 && j >= 1) bf[7 + 3 * st + j] = ldws(rs, hb + (7 + 3 * st + j) * 1024);
                        if constexpr (st == 6 && (j & 1)) bf[14 + ks] = ldws(rs, hb + (14 + ks) * 1024);
                        if constexpr (st >= 8 && st <= 11 && (j & 1)) bf[ks - 16] = ldws(rs, ob + (ks - 16) * 1024);
                        if constexpr (st == 14 || st == 15) {
                            constexpr int nh = st - 14;
                            *(LAS f32x4*)(fd + nh * 128 + j * 32) = f32x4{af[nh][4 * j], af[nh][4 * j + 1], af[nh][4 * j + 2], af[nh][4 * j + 3]};
                        }
                        if (j == 3) phook(st);
                    });
        BAR();
        {
            lds_ptr d = sm + (kq < 3 ? simg(kq) + mh * 32 * 272 : mh ? 151552 : 0) + (lane & 31) * 272 + hi * 16;
#pragma unroll
            for (int nh = 0; nh < 2; ++nh)
#pragma unroll
                for (int c = 0; c < 4; ++c)
                    *(LAS f32x4*)(d + nh * 128 + c * 32) = f32x4{as[nh][4 * c], as[nh][4 * c + 1], as[nh][4 * c + 2], as[nh][4 * c + 3]};
        }
        lds_ptr d = sm + (tid >> 3) * 272 + (tid & 7) * 32;
        float gt[8];
        {
            f32x4 f0 = {}, f1 = {};
#pragma unroll
            for (int k = 0; k < 4; ++k) { f0 += *(LAS f32x4*)(d + fimg(k)); f1 += *(LAS f32x4*)(d + fimg(k) + 16); }
#pragma unroll
            for (int e = 0; e < 8; ++e) {
                const float f = rbf(e < 4 ? f0[e & 3] : f1[e & 3]);
                gt[e] = rbf(f * __builtin_amdgcn_rcpf(1.0f + __builtin_amdgcn_exp2f(-1.4426950408889634f * f)));
                asm volatile("" : "+v"(gt[e]));
            }
        }
        BAR();
        {
            f32x4 s0 = {}, s1 = {};
#pragma unroll
            for (int k = 0; k < 4; ++k) {
                const int so = k < 3 ? simg(k) : (w >= 4 ? 151552 - 32 * 272 : 0);
                s0 += *(LAS f32x4*)(d + so); s1 += *(LAS f32x4*)(d + so + 16);
            }
            float r[8];
#pragma unroll
            for (int e = 0; e < 8; ++e) {
                const float xv = (e & 1) ? bfhi(xres[e >> 1]) : bflo(xres[e >> 1]);
                r[e] = __builtin_fmaf(e < 4 ? s0[e & 3] : s1[e & 3], gt[e], xv);
            }
            u32x4 ov = {pk2(r[0], r[1]), pk2(r[2], r[3]), pk2(r[4], r[5]), pk2(r[6], r[7])};
            const __amdgpu_buffer_rsrc_t ro = __builtin_amdgcn_make_buffer_rsrc((void*)p.out, 0, 2048 * 896, 0x00020000);
            stws(ro, (rb * 64 + (tid >> 3)) * 896 + cb * 128 + (tid & 7) * 16, ov);
            STAMP(26); STAMP_OUT;
        }
    }
}

extern "C" void kernel_launch(void* const* d_in, const int* in_sizes, int n_in, void* d_out, int out_size, void* d_ws, size_t ws_size,
                              hipStream_t stream) {
    (void)in_sizes; (void)n_in; (void)out_size; (void)ws_size;
    Params p;
    p.x = (const char*)d_in[0]; p.wqkv = (const char*)d_in[1]; p.wo = (const char*)d_in[2];
    p.wg = (const char*)d_in[4]; p.wu = (const char*)d_in[5]; p.wd = (const char*)d_in[6];
    p.out = (char*)d_out; p.ws = (char*)d_ws;
    (void)hipFuncSetAttribute((const void*)fused_kernel, hipFuncAttributeMaxDynamicSharedMemorySize, LDS_BYTES);
    fused_kernel<<<256, 512, LDS_BYTES, stream>>>(p);
}
```
